```python
import torch

def _softmax(x, axis=-1):
    return torch.softmax(x, dim=axis)
def _gelu(x):
    return torch.nn.functional.gelu(x, approximate="tanh")
def _silu(x):
    return torch.nn.functional.silu(x)
def _sigmoid(x):
    return torch.sigmoid(x)
def _layernorm(x, axis=-1, eps=1e-5):
    m = x.mean(dim=axis, keepdim=True)
    v = x.var(dim=axis, keepdim=True, unbiased=False)
    return (x - m) / torch.sqrt(v + eps)
def _scatter_add(tgt, idx, src, axis=0):
    return tgt.clone().index_add_(axis, idx, src)
def _segment_sum(src, seg, n):
    out = torch.zeros((n,) + src.shape[1:], dtype=src.dtype)
    return out.index_add_(0, seg, src)
def _pad(x, pw):
    flat = tuple(v for pair in reversed(pw) for v in pair)
    return torch.nn.functional.pad(x, flat)

kernel_name = 'standalone__gqa_block_cached__decode'

def setup_inputs():

    _w = (lambda a: (lambda t: t.to(torch.bfloat16) if t.is_floating_point() else t)(torch.as_tensor(a)))
    B, Sq, D = 1, 1, 448
    Hq, Hkv, Sk, Dh = 16, 8, 8192, 256
    assert Hq % Hkv == 0, (Hq, Hkv)
    return {
        "x": _w((lambda *s: torch.randn(*s, dtype=torch.bfloat16) * 0.8)(B, Sq, D)),
        "k_cache": _w((lambda *s: torch.randn(*s, dtype=torch.bfloat16) * 0.8)(B, Hkv, Sk, Dh)),
        "v_cache": _w((lambda *s: torch.randn(*s, dtype=torch.bfloat16) * 0.05)(B, Hkv, Sk, Dh)),
        "wq": _w((lambda *s: torch.randn(*s, dtype=torch.bfloat16) * 0.8)(D, Hq * Dh) * (D ** -0.5)),
        "wo": _w((lambda *s: torch.randn(*s, dtype=torch.bfloat16) * 0.05)(Hq * Dh, D) * ((Hq * Dh) ** -0.5)),
    }

def reference(x, k_cache, v_cache, wq, wo):
    B, Sq, D = x.shape
    Hq, Dh = 16, 256
    Hkv = k_cache.shape[1]
    rep = Hq // Hkv
    q = (lambda x, *d: x.permute(*d))((x @ wq).reshape(B, Sq, Hq, Dh), 0, 2, 1, 3)
    k = (lambda x, n, axis: torch.repeat_interleave(x, n, dim=axis))(k_cache, rep, axis=1)
    v = (lambda x, n, axis: torch.repeat_interleave(x, n, dim=axis))(v_cache, rep, axis=1)
    scores = torch.einsum("bhqd,bhkd->bhqk", q, k) * (Dh ** -0.5)
    p = _softmax(scores, axis=-1)
    o = torch.einsum("bhqk,bhkd->bhqd", p, v)
    return (lambda x, *d: x.permute(*d))(o, 0, 2, 1, 3).reshape(B, Sq, Hq * Dh) @ wo
```

```cpp
#include <hip/hip_runtime.h>
#include <hip/hip_bf16.h>
#include <stdint.h>

typedef unsigned u32x4 __attribute__((ext_vector_type(4)));
typedef unsigned u32x2 __attribute__((ext_vector_type(2)));
typedef float f32x4 __attribute__((ext_vector_type(4)));
typedef __bf16 bf16x8 __attribute__((ext_vector_type(8)));
#define GAS __attribute__((address_space(1)))
#define LAS __attribute__((address_space(3)))

constexpr int DM = 448, HQ = 16, HKV = 8, SK = 8192, DH = 256, NQ = HQ * DH;
constexpr int NSPLIT = 32, KEYS = SK / NSPLIT;

__device__ __forceinline__ float bf2f(unsigned short b) { return __builtin_bit_cast(float, (unsigned)b << 16); }
__device__ __forceinline__ unsigned cvtpk(float lo, float hi) { unsigned r; asm volatile("v_cvt_pk_bf16_f32 %0, %1, %2" : "=v"(r) : "v"(lo), "v"(hi)); return r; }
__device__ __forceinline__ f32x4 mfma32(u32x4 a, u32x4 b, f32x4 c) {
    return __builtin_amdgcn_mfma_f32_16x16x32_bf16(__builtin_bit_cast(bf16x8, a), __builtin_bit_cast(bf16x8, b), c, 0, 0, 0);
}

__global__ __launch_bounds__(64) void k_qproj(const unsigned short* __restrict__ x, const unsigned short* __restrict__ wq, unsigned short* __restrict__ q) {
    const int j = blockIdx.x, g = j & 7, s = j >> 3, l = threadIdx.x;
    const int c0 = 512 * g + 16 * s + 8 * (l & 1), r0 = 14 * (l >> 1);
    float acc[8] = {0, 0, 0, 0, 0, 0, 0, 0};
    u32x4 w[14];
#pragma unroll
    for (int i = 0; i < 14; ++i) w[i] = *(const u32x4*)(wq + (size_t)(r0 + i) * NQ + c0);
#pragma unroll
    for (int i = 0; i < 14; ++i) {
        const float xv = bf2f(x[r0 + i]);
#pragma unroll
        for (int k = 0; k < 4; ++k) {
            acc[2 * k] = fmaf(xv, __builtin_bit_cast(float, w[i][k] << 16), acc[2 * k]);
            acc[2 * k + 1] = fmaf(xv, __builtin_bit_cast(float, w[i][k] & 0xffff0000u), acc[2 * k + 1]);
        }
    }
#pragma unroll
    for (int m = 2; m < 64; m <<= 1)
#pragma unroll
        for (int k = 0; k < 8; ++k) acc[k] += __shfl_xor(acc[k], m);
    if (l < 2) {
        u32x4 o = {cvtpk(acc[0], acc[1]), cvtpk(acc[2], acc[3]), cvtpk(acc[4], acc[5]), cvtpk(acc[6], acc[7])};
        *(u32x4*)(q + c0) = o;
    }
}

constexpr int LDS_V = 0, LDS_P = 131072, LDS_M = LDS_P + 8192, LDS_L = LDS_M + 64, LDS_TOTAL = LDS_L + 64;

#define KLOAD(dst, voff, sbase, imm) asm volatile("global_load_dwordx4 %0, %1, %2 offset:" #imm " nt" : "=v"(dst) : "v"(voff), "s"(sbase) : "memory")
__device__ __forceinline__ void vdma(unsigned voff, const void* sbase, unsigned m0v) {
    asm volatile("s_mov_b32 m0, %2\n\ts_nop 0\n\tglobal_load_lds_dwordx4 %0, %1 nt" :: "v"(voff), "s"(sbase), "s"(m0v) : "memory");
}
#define VMWAIT(n) asm volatile("s_waitcnt vmcnt(" #n ")" ::: "memory")
#define TRRD(dst, addr, off) asm volatile("ds_read_b64_tr_b16 %0, %1 offset:%2" : "=v"(dst) : "v"(addr), "i"(off) : "memory")

__global__ __launch_bounds__(512, 2) void k_attn(const unsigned short* __restrict__ Kc, const unsigned short* __restrict__ Vc,
                                                 const unsigned short* __restrict__ q, float* __restrict__ part, float* __restrict__ ml) {
    extern __shared__ __attribute__((aligned(16))) unsigned char smem[];
    const int tid = threadIdx.x, l = tid & 63, w = __builtin_amdgcn_readfirstlane(tid >> 6);
    const int g = blockIdx.x & 7, s = blockIdx.x >> 3;
    const int gq = l >> 4, c = l & 15;
    const unsigned ldsb = (unsigned)(size_t)(LAS unsigned char*)smem;
    const char* Kb = (const char*)Kc + ((size_t)g * SK + (size_t)s * KEYS) * 512;
    const char* Vb = (const char*)Vc + ((size_t)g * SK + (size_t)s * KEYS) * 512;
    u32x4 qf[8];
    {
        const unsigned short* qp = q + (2 * g + ((c >> 2) & 1)) * DH + 8 * gq;
#pragma unroll
        for (int ds = 0; ds < 8; ++ds) qf[ds] = *(const u32x4*)(qp + 32 * ds);
#pragma unroll
        for (int ds = 0; ds < 8; ++ds) asm volatile("" : "+v"(qf[ds]));
    }
    const unsigned ko0 = (unsigned)((32 * w + c) * 512 + gq * 16), ko1 = ko0 + 8192;
    u32x4 kf[16];
    KLOAD(kf[0], ko0, Kb, 0);   KLOAD(kf[1], ko0, Kb, 64);  KLOAD(kf[2], ko0, Kb, 128); KLOAD(kf[3], ko0, Kb, 192);
    KLOAD(kf[4], ko0, Kb, 256); KLOAD(kf[5], ko0, Kb, 320); KLOAD(kf[6], ko0, Kb, 384); KLOAD(kf[7], ko0, Kb, 448);
    KLOAD(kf[8], ko1, Kb, 0);   KLOAD(kf[9], ko1, Kb, 64);  KLOAD(kf[10], ko1, Kb, 128); KLOAD(kf[11], ko1, Kb, 192);
    KLOAD(kf[12], ko1, Kb, 256); KLOAD(kf[13], ko1, Kb, 320); KLOAD(kf[14], ko1, Kb, 384); KLOAD(kf[15], ko1, Kb, 448);
    const unsigned vo = (unsigned)((l >> 2) * 512 + w * 64 + (((l & 3) ^ (((l >> 4) & 1) << 1)) * 16));
#pragma unroll
    for (int i = 0; i < 16; ++i) vdma(vo, Vb + (size_t)i * 8192, ldsb + LDS_V + w * 16384 + i * 1024);
    asm volatile("s_waitcnt vmcnt(16)" : "+v"(kf[0]), "+v"(kf[1]), "+v"(kf[2]), "+v"(kf[3]), "+v"(kf[4]), "+v"(kf[5]), "+v"(kf[6]), "+v"(kf[7]) :: "memory");
    asm volatile("" : "+v"(kf[8]), "+v"(kf[9]), "+v"(kf[10]), "+v"(kf[11]), "+v"(kf[12]), "+v"(kf[13]), "+v"(kf[14]), "+v"(kf[15]) :: "memory");
    f32x4 s0 = {0, 0, 0, 0}, s1 = {0, 0, 0, 0};
#pragma unroll
    for (int ds = 0; ds < 8; ++ds) { s0 = mfma32(kf[ds], qf[ds], s0); s1 = mfma32(kf[8 + ds], qf[ds], s1); }
    constexpr float C2 = 0.0625f * 1.4426950408889634f;
    float sv[8] = {s0[0] * C2, s0[1] * C2, s0[2] * C2, s0[3] * C2, s1[0] * C2, s1[1] * C2, s1[2] * C2, s1[3] * C2};
    float mx = fmaxf(fmaxf(fmaxf(sv[0], sv[1]), fmaxf(sv[2], sv[3])), fmaxf(fmaxf(sv[4], sv[5]), fmaxf(sv[6], sv[7])));
    mx = fmaxf(mx, __shfl_xor(mx, 16)); mx = fmaxf(mx, __shfl_xor(mx, 32));
    LAS float* lm = (LAS float*)(size_t)(ldsb + LDS_M);
    LAS float* ll = (LAS float*)(size_t)(ldsb + LDS_L);
    const int hd = (c >> 2) & 1;
    if (l == 0 || l == 4) lm[hd * 8 + w] = mx;
    asm volatile("s_waitcnt lgkmcnt(0)\n\ts_barrier" ::: "memory");
    float M = lm[hd * 8];
#pragma unroll
    for (int i = 1; i < 8; ++i) M = fmaxf(M, lm[hd * 8 + i]);
    float p[8], ls = 0.f;
#pragma unroll
    for (int i = 0; i < 8; ++i) { p[i] = __builtin_amdgcn_exp2f(sv[i] - M); ls += p[i]; }
    ls += __shfl_xor(ls, 16); ls += __shfl_xor(ls, 32);
    if (l == 0 || l == 4) ll[hd * 8 + w] = ls;
    u32x4 pf = {cvtpk(p[0], p[1]), cvtpk(p[2], p[3]), cvtpk(p[4], p[5]), cvtpk(p[6], p[7])};
    *(LAS u32x4*)(size_t)(ldsb + LDS_P + w * 1024 + l * 16) = pf;
    asm volatile("s_waitcnt lgkmcnt(0)\n\ts_barrier" ::: "memory");
    u32x4 pa[8];
#pragma unroll
    for (int ks = 0; ks < 8; ++ks) pa[ks] = *(LAS u32x4*)(size_t)(ldsb + LDS_P + ks * 1024 + l * 16);
    if (tid == 0 || tid == 4) {
        float L = 0.f;
#pragma unroll
        for (int i = 0; i < 8; ++i) L += ll[hd * 8 + i];
        float* o = ml + ((2 * g + hd) * NSPLIT + s) * 2;
        o[0] = M; o[1] = L;
    }
    const int qq = c >> 2, pp = l & 3;
    const unsigned a0 = ldsb + LDS_V + w * 16384 + (4 * gq + qq) * 64 + ((((gq & 1) << 1) | (pp >> 1)) * 16) + 8 * (pp & 1), a1 = a0 ^ 32;
    f32x4 o0 = {0, 0, 0, 0}, o1 = {0, 0, 0, 0};
    VMWAIT(0);
#pragma unroll
    for (int ks = 0; ks < 8; ++ks) {
        u32x2 b00, b01, b10, b11;
        TRRD(b00, a0, ks * 2048); TRRD(b01, a0, ks * 2048 + 1024); TRRD(b10, a1, ks * 2048); TRRD(b11, a1, ks * 2048 + 1024);
        asm volatile("s_waitcnt lgkmcnt(0)" : "+v"(b00), "+v"(b01), "+v"(b10), "+v"(b11) :: "memory");
        __builtin_amdgcn_sched_barrier(0);
        u32x4 b0 = {b00[0], b00[1], b01[0], b01[1]}, b1 = {b10[0], b10[1], b11[0], b11[1]};
        o0 = mfma32(pa[ks], b0, o0); o1 = mfma32(pa[ks], b1, o1);
    }
    const float ov = (gq >> 1) ? o1[0] : o0[0];
    part[((size_t)(2 * g + (gq & 1)) * NSPLIT + s) * DH + 32 * w + 16 * (gq >> 1) + c] = ov;
}

__global__ __launch_bounds__(64) void k_comb(const float* __restrict__ part, const float* __restrict__ ml, const unsigned short* __restrict__ wo, float* __restrict__ op) {
    const int j = blockIdx.x, h = j >> 4, db = j & 15, l = threadIdx.x;
    __shared__ float o[16];
    const float m = ml[(h * NSPLIT + (l & 31)) * 2], ls = ml[(h * NSPLIT + (l & 31)) * 2 + 1];
    float M = m;
#pragma unroll
    for (int k = 1; k < 32; k <<= 1) M = fmaxf(M, __shfl_xor(M, k));
    const float wt = __builtin_amdgcn_exp2f(m - M);
    float L = wt * ls;
#pragma unroll
    for (int k = 1; k < 32; k <<= 1) L += __shfl_xor(L, k);
    const float wn = wt / L;
    float acc = 0.f;
#pragma unroll
    for (int i = 0; i < 8; ++i) { const int sp = (l >> 4) + 4 * i; acc = fmaf(__shfl(wn, sp), part[((size_t)h * NSPLIT + sp) * DH + 16 * db + (l & 15)], acc); }
    acc += __shfl_xor(acc, 16); acc += __shfl_xor(acc, 32);
    if (l < 16) { unsigned r = cvtpk(acc, acc); o[l] = bf2f((unsigned short)(r & 0xffff)); }
    __syncthreads();
    if (l < 56) {
        float a[8] = {0, 0, 0, 0, 0, 0, 0, 0};
#pragma unroll
        for (int d = 0; d < 16; ++d) {
            const u32x4 wv = *(const u32x4*)(wo + (size_t)(256 * h + 16 * db + d) * DM + 8 * l);
            const float ov = o[d];
#pragma unroll
            for (int k = 0; k < 4; ++k) { a[2 * k] = fmaf(ov, __builtin_bit_cast(float, wv[k] << 16), a[2 * k]); a[2 * k + 1] = fmaf(ov, __builtin_bit_cast(float, wv[k] & 0xffff0000u), a[2 * k + 1]); }
        }
#pragma unroll
        for (int k = 0; k < 8; ++k) op[(size_t)j * DM + 8 * l + k] = a[k];
    }
}

__global__ __launch_bounds__(256) void k_final(const float* __restrict__ op, unsigned short* __restrict__ out) {
    __shared__ float red[256];
    const int b = blockIdx.x, t = threadIdx.x, cidx = 16 * b + (t & 15);
    float a = 0.f;
#pragma unroll
    for (int i = 0; i < 16; ++i) a += op[(size_t)((t >> 4) * 16 + i) * DM + cidx];
    red[t] = a;
    __syncthreads();
    if (t < 16) { float sum = 0.f;
#pragma unroll
        for (int i = 0; i < 16; ++i) sum += red[i * 16 + t];
        out[cidx] = (unsigned short)(cvtpk(sum, sum) & 0xffff); }
}

extern "C" void kernel_launch(void* const* d_in, const int* in_sizes, int n_in, void* d_out, int out_size, void* d_ws, size_t ws_size, hipStream_t stream) {
    const unsigned short* x = (const unsigned short*)d_in[0];
    const unsigned short* kc = (const unsigned short*)d_in[1];
    const unsigned short* vc = (const unsigned short*)d_in[2];
    const unsigned short* wq = (const unsigned short*)d_in[3];
    const unsigned short* wo = (const unsigned short*)d_in[4];
    char* ws = (char*)d_ws;
    unsigned short* q = (unsigned short*)ws;
    float* ml = (float*)(ws + 16384);
    float* part = (float*)(ws + 65536);
    float* op = (float*)(ws + 65536 + 524288);
    hipFuncSetAttribute((const void*)k_attn, hipFuncAttributeMaxDynamicSharedMemorySize, LDS_TOTAL);
    k_qproj<<<256, 64, 0, stream>>>(x, wq, q);
    k_attn<<<256, 512, LDS_TOTAL, stream>>>(kc, vc, q, part, ml);
    k_comb<<<256, 64, 0, stream>>>(part, ml, wo, op);
    k_final<<<28, 256, 0, stream>>>(op, (unsigned short*)d_out);
}
```

```cpp
#include <hip/hip_runtime.h>
#include <stdint.h>

typedef unsigned u32x4 __attribute__((ext_vector_type(4)));
typedef unsigned u32x2 __attribute__((ext_vector_type(2)));
typedef float f32x4 __attribute__((ext_vector_type(4)));
typedef __bf16 bf16x8 __attribute__((ext_vector_type(8)));
typedef short s16x4 __attribute__((ext_vector_type(4)));
#define GAS __attribute__((address_space(1)))
#define LAS __attribute__((address_space(3)))
typedef GAS unsigned long long gu64;
typedef GAS unsigned gu32;
#define RLX_AGENT __ATOMIC_RELAXED, __HIP_MEMORY_SCOPE_AGENT

#define WIN 16
#define QSLEEP 16
#define RDELAY 16
#define QDELAY 8
#define KV_NT 1

constexpr int DM = 448, SK = 8192, NQ = 4096;
constexpr int NWAVE = 11, NTHR = NWAVE * 64;
constexpr int WS_E = 0, WS_RDY = 4096, WS_Q = 8192, WS_PART = WS_Q + 65536, WS_OUTP = WS_PART + 1048576;
constexpr int WS_END = WS_OUTP + 256 * DM * 4;
constexpr int L_SLOT = 0, L_WO = 131072, L_P = L_WO + 14336, L_Q = L_P + 8192, L_X = L_Q + 1152, L_M = L_X + 1024, L_L = L_M + 64, L_FLAG = L_L + 64, LDS_TOTAL = L_FLAG + 256;
constexpr int F_LAND = 0, F_KREL = 16, F_QRDY = 26, F_PCNT = 27, F_PUB = 28, F_NUM = 32;
constexpr unsigned SENT = 0xFFFFFFFFu;
constexpr int GCAP = 1 << 15, LCAP = 1 << 20;

__device__ __forceinline__ float bf2f(unsigned b) { return __builtin_bit_cast(float, b << 16); }
typedef __bf16 bf16x2 __attribute__((ext_vector_type(2)));
__device__ __forceinline__ unsigned cvtpk(float lo, float hi) { bf16x2 v = {(__bf16)lo, (__bf16)hi}; return __builtin_bit_cast(unsigned, v); }
__device__ __forceinline__ f32x4 mfma32(u32x4 a, u32x4 b, f32x4 c) {
    return __builtin_amdgcn_mfma_f32_16x16x32_bf16(__builtin_bit_cast(bf16x8, a), __builtin_bit_cast(bf16x8, b), c, 0, 0, 0);
}
template <int CTRL> __device__ __forceinline__ float dpp(float x) {
    return __builtin_bit_cast(float, __builtin_amdgcn_mov_dpp(__builtin_bit_cast(int, x), CTRL, 0xf, 0xf, true));
}
constexpr int XOR1 = 0xB1, XOR2 = 0x4E, HMIR = 0x141, MIR = 0x140, ROR4 = 0x124, ROR8 = 0x128;
__device__ __forceinline__ float row_sum(float x) { x += dpp<XOR1>(x); x += dpp<XOR2>(x); x += dpp<HMIR>(x); x += dpp<MIR>(x); return x; }
__device__ __forceinline__ float row_max(float x) { x = fmaxf(x, dpp<XOR1>(x)); x = fmaxf(x, dpp<XOR2>(x)); x = fmaxf(x, dpp<HMIR>(x)); x = fmaxf(x, dpp<MIR>(x)); return x; }
__device__ __forceinline__ float swap32_add(float a, float b) { auto t = __builtin_amdgcn_permlane32_swap(__float_as_uint(a), __float_as_uint(b), false, false); return __uint_as_float(t[0]) + __uint_as_float(t[1]); }
__device__ __forceinline__ float swap16_add(float a, float b) { auto t = __builtin_amdgcn_permlane16_swap(__float_as_uint(a), __float_as_uint(b), false, false); return __uint_as_float(t[0]) + __uint_as_float(t[1]); }
__device__ __forceinline__ float swap32_max(float a) { auto t = __builtin_amdgcn_permlane32_swap(__float_as_uint(a), __float_as_uint(a), false, false); return fmaxf(__uint_as_float(t[0]), __uint_as_float(t[1])); }
__device__ __forceinline__ float swap16_max(float a) { auto t = __builtin_amdgcn_permlane16_swap(__float_as_uint(a), __float_as_uint(a), false, false); return fmaxf(__uint_as_float(t[0]), __uint_as_float(t[1])); }

template <int NT> __device__ __forceinline__ void dma(unsigned voff, const void* sbase, unsigned m0v) {
    if (NT) asm volatile("s_mov_b32 m0, %2\n\ts_nop 0\n\tglobal_load_lds_dwordx4 %0, %1 nt" :: "v"(voff), "s"(sbase), "s"(m0v) : "memory");
    else asm volatile("s_mov_b32 m0, %2\n\ts_nop 0\n\tglobal_load_lds_dwordx4 %0, %1" :: "v"(voff), "s"(sbase), "s"(m0v) : "memory");
}
template <int N> __device__ __forceinline__ void vmwait() { asm volatile("s_waitcnt vmcnt(%0)" :: "i"(N) : "memory"); }
__device__ __forceinline__ void lds_wait_ge(volatile LAS unsigned* p, int want, bool slp) {
#pragma unroll 1
    for (int it = 0; it < LCAP; ++it) { if (__builtin_amdgcn_readfirstlane((int)*p) >= want) break; if (slp) __builtin_amdgcn_s_sleep(1); }
    asm volatile("" ::: "memory");
}
__device__ __forceinline__ void st16_sc1(const void* sbase, unsigned voff, u32x4 v) { asm volatile("global_store_dwordx4 %0, %1, %2 sc1\n\ts_nop 1" :: "v"(voff), "v"(v), "s"(sbase) : "memory"); }
__device__ __forceinline__ void st8_sc1(const void* sbase, unsigned voff, u32x2 v) { asm volatile("global_store_dwordx2 %0, %1, %2 sc1" :: "v"(voff), "v"(v), "s"(sbase) : "memory"); }
__device__ __forceinline__ void st4_sc1(const void* sbase, unsigned voff, unsigned v) { asm volatile("global_store_dword %0, %1, %2 sc1" :: "v"(voff), "v"(v), "s"(sbase) : "memory"); }
__device__ __forceinline__ s16x4 trread(unsigned addr) { return __builtin_amdgcn_ds_read_tr16_b64_v4i16((LAS s16x4*)(size_t)addr); }
__device__ __forceinline__ u32x4 cat2(s16x4 a, s16x4 b) { u32x2 x = __builtin_bit_cast(u32x2, a), y = __builtin_bit_cast(u32x2, b); return u32x4{x[0], x[1], y[0], y[1]}; }

#define LDS_ALLOC LDS_TOTAL
#define STAMP(i) do {} while (0)
#define STAMP_FLUSH() do {} while (0)
#define STAMP_INIT() do {} while (0)

#define STR_(x) #x
#define XSTR(x) STR_(x)
#define KVNT " nt"

__global__ __launch_bounds__(NTHR) void fused_decode(const unsigned short* __restrict__ x, const unsigned short* __restrict__ Kc, const unsigned short* __restrict__ Vc,
                                                     const unsigned short* __restrict__ wq, const unsigned short* __restrict__ wo, unsigned short* __restrict__ out, unsigned char* ws) {
    extern __shared__ __attribute__((aligned(16))) unsigned char smem[];
    const int tid = threadIdx.x, lane = tid & 63, wave = __builtin_amdgcn_readfirstlane(tid >> 6);
    const int g = blockIdx.x & 7, s = blockIdx.x >> 3, blk = g * 32 + s;
    const unsigned ldsb = (unsigned)(size_t)(LAS unsigned char*)smem;
    volatile LAS unsigned* flag = (volatile LAS unsigned*)(size_t)(ldsb + L_FLAG);
    const int gq = lane >> 4, c16 = lane & 15;
    STAMP_INIT();
    STAMP(0);
    if (wave < 2) {
        const int L = wave, hi = lane >> 5, l31 = lane & 31;
        const char* Kb = (const char*)Kc + (size_t)blk * 131072 + L * 16384;
        const char* Vb = (const char*)Vc + (size_t)blk * 131072 + L * 16384;
        const int hh = s >> 4, db = s & 15;
        const char* Wb = (const char*)wo + (size_t)((2 * g + hh) * 256 + 16 * db) * (DM * 2) + L * 64;
        const unsigned voff = (lane >> 3) * 512 + (((lane & 7) ^ (lane >> 3)) * 16);
        const unsigned wof = (l31 >> 1) * (DM * 2) + hi * 32 + (l31 & 1) * 16;
        const unsigned sl = ldsb + L_SLOT + L * 16384;
        const unsigned vland = ldsb + L_FLAG + (F_LAND + L) * 4, vrel = ldsb + L_FLAG + (F_KREL + L) * 4;
        asm volatile(
            "s_mov_b32 s40, 0\n\t"
            "s_mov_b64 s[42:43], %[kb]\n\t"
            "s_mov_b32 s44, 0-" XSTR(WIN) "\n\t"
            "v_mov_b32 v20, %[vrel]\n\t"
            "v_mov_b32 v21, %[vland]\n\t"
            "1:\n\t"
            "s_mov_b32 s45, %[sl]\n\t"
            "s_mov_b32 s41, 4\n\t"
            "2:\n\t"
            "s_cmp_eq_u32 s40, 0\n\t"
            "s_cbranch_scc1 4f\n\t"
            "s_mov_b32 s46, 0x100000\n\t"
            "3:\n\t"
            "ds_read_b32 v22, v20\n\t"
            "s_waitcnt lgkmcnt(0)\n\t"
            "v_readfirstlane_b32 s47, v22\n\t"
            "s_sub_u32 s46, s46, 1\n\t"
            "s_cbranch_scc1 5f\n\t"
            "s_cmp_eq_u32 s47, 0\n\t"
            "s_cbranch_scc1 3b\n\t"
            "5:\n\t"
            "v_add_u32 v20, 8, v20\n\t"
            "4:\n\t"
            "s_mov_b32 s46, 4\n\t"
            "6:\n\t"
            "s_mov_b32 m0, s45\n\t"
            "s_add_u32 s45, s45, 4096\n\t"
            "global_load_lds_dwordx4 %[voff], s[42:43]" KVNT "\n\t"
            "s_addk_i32 m0, 896\n\t"
            "s_waitcnt vmcnt(" XSTR(WIN) ")\n\t"
            "global_load_lds_dwordx4 %[voff], s[42:43] offset:128" KVNT "\n\t"
            "s_addk_i32 m0, 896\n\t"
            "s_waitcnt vmcnt(" XSTR(WIN) ")\n\t"
            "global_load_lds_dwordx4 %[voff], s[42:43] offset:256" KVNT "\n\t"
            "s_addk_i32 m0, 896\n\t"
            "s_waitcnt vmcnt(" XSTR(WIN) ")\n\t"
            "global_load_lds_dwordx4 %[voff], s[42:43] offset:384" KVNT "\n\t"
            "s_add_u32 s42, s42, 4096\n\t"
            "s_addc_u32 s43, s43, 0\n\t"
            "s_add_i32 s44, s44, 4\n\t"
            "v_mov_b32 v22, s44\n\t"
            "s_waitcnt vmcnt(" XSTR(WIN) ")\n\t"
            "ds_write_b32 v21, v22\n\t"
            "s_cmp_lg_u32 s44, 0\n\t"
            "s_cbranch_scc1 7f\n\t"
            "s_barrier\n\t"
            "ds_write_b32 v21, v22\n\t"
            "7:\n\t"
            "s_sub_u32 s46, s46, 1\n\t"
            "s_cmp_lg_u32 s46, 0\n\t"
            "s_cbranch_scc1 6b\n\t"
            "s_add_u32 s42, s42, 16384\n\t"
            "s_addc_u32 s43, s43, 0\n\t"
            "s_add_u32 s45, s45, 16384\n\t"
            "s_sub_u32 s41, s41, 1\n\t"
            "s_cmp_lg_u32 s41, 0\n\t"
            "s_cbranch_scc1 2b\n\t"
            "s_mov_b64 s[42:43], %[vb]\n\t"
            "s_add_u32 s40, s40, 1\n\t"
            "s_cmp_eq_u32 s40, 1\n\t"
            "s_cbranch_scc1 1b\n\t"
            "s_mov_b64 s[42:43], %[wb]\n\t"
            "s_mov_b32 m0, %[lwo]\n\t"
            "s_mov_b32 s46, 7\n\t"
            "8:\n\t"
            "global_load_lds_dwordx4 %[wof], s[42:43]\n\t"
            "s_addk_i32 m0, 2048\n\t"
            "s_add_u32 s42, s42, 128\n\t"
            "s_addc_u32 s43, s43, 0\n\t"
            "s_sub_u32 s46, s46, 1\n\t"
            "s_cmp_lg_u32 s46, 0\n\t"
            "s_cbranch_scc1 8b\n\t"
            ".if 128-" XSTR(WIN) " < 80\n\ts_waitcnt vmcnt(55)\n\tv_mov_b32 v22, 80\n\tds_write_b32 v21, v22\n\t.endif\n\t"
            ".if 128-" XSTR(WIN) " < 96\n\ts_waitcnt vmcnt(39)\n\tv_mov_b32 v22, 96\n\tds_write_b32 v21, v22\n\t.endif\n\t"
            ".if 128-" XSTR(WIN) " < 112\n\ts_waitcnt vmcnt(23)\n\tv_mov_b32 v22, 112\n\tds_write_b32 v21, v22\n\t.endif\n\t"
            "s_waitcnt vmcnt(7)\n\tv_mov_b32 v22, 128\n\tds_write_b32 v21, v22\n\t"
            "s_waitcnt vmcnt(0)\n\tv_mov_b32 v22, 135\n\tds_write_b32 v21, v22\n\t"
            "s_waitcnt lgkmcnt(0)\n\t"
            :: [kb] "s"(Kb), [vb] "s"(Vb), [wb] "s"(Wb), [sl] "s"(sl), [lwo] "s"(ldsb + L_WO + L * 1024), [voff] "v"(voff), [wof] "v"(wof), [vrel] "s"(vrel), [vland] "s"(vland)
            : "s40", "s41", "s42", "s43", "s44", "s45", "s46", "s47", "v20", "v21", "v22", "scc", "memory");
        STAMP(3);
    } else if (wave == 2) {
        if (lane < F_NUM) flag[lane] = 0u;
        const unsigned long long E = __hip_atomic_load((gu64*)(ws + WS_E), RLX_AGENT);
        {
            const int cbk = s >> 1, rh = s & 1;
            const char* wqb = (const char*)wq + (size_t)(224 * rh) * (NQ * 2) + (512 * g + 32 * cbk) * 2;
            const unsigned wqo = (lane >> 2) * (NQ * 2) + (lane & 3) * 16;
#pragma unroll 1
            for (int i = 0; i < 14; ++i) dma<0>(wqo, wqb + (size_t)i * 16 * (NQ * 2), ldsb + L_WO + i * 1024);
            if (lane < 28) dma<0>(lane * 16, (const char*)x + rh * 448, ldsb + L_X);
        }
        {
            const u32x4 sv = {SENT, SENT, SENT, SENT};
#pragma unroll
            for (int i = 0; i < 4; ++i) st16_sc1(ws, WS_PART + blk * 4096 + i * 1024 + lane * 16, sv);
            st16_sc1(ws, WS_OUTP + blk * (DM * 4) + lane * 16, sv);
            if (lane < 48) st16_sc1(ws, WS_OUTP + blk * (DM * 4) + 1024 + lane * 16, sv);
        }
        asm volatile("s_waitcnt lgkmcnt(0)\n\ts_barrier" ::: "memory");
        asm volatile("s_waitcnt vmcnt(0)" ::: "memory");
        STAMP(1);
        float qv;
        {
            const unsigned wa = ldsb + L_WO + (8 * gq + (c16 >> 2)) * 64 + 8 * (lane & 3), xa = ldsb + L_X + 16 * gq;
            u32x4 bw[14], ax[7];
#pragma unroll
            for (int ks = 0; ks < 7; ++ks) {
                bw[2 * ks] = cat2(trread(wa + ks * 2048), trread(wa + ks * 2048 + 256));
                bw[2 * ks + 1] = cat2(trread(wa + ks * 2048 + 32), trread(wa + ks * 2048 + 256 + 32));
                ax[ks] = *(LAS u32x4*)(size_t)(xa + ks * 64);
            }
            f32x4 d0 = {0, 0, 0, 0}, d1 = {0, 0, 0, 0};
#pragma unroll
            for (int ks = 0; ks < 7; ++ks) { d0 = mfma32(ax[ks], bw[2 * ks], d0); d1 = mfma32(ax[ks], bw[2 * ks + 1], d1); }
            qv = (gq & 1) ? d1[0] : d0[0];
        }
        const unsigned long long tag = E + 1ull;
        const unsigned tagl = (unsigned)tag & 0xffu, tagh = (unsigned)(tag >> 8);
        if (lane < 32) st8_sc1(ws, WS_Q + g * 8192 + s * 256 + lane * 8, u32x2{((__float_as_uint(qv) + 0x80u) & 0xffffff00u) | tagl, tagh});
        asm volatile("s_waitcnt vmcnt(0)" ::: "memory");
        STAMP(6);
        __builtin_amdgcn_s_sleep(QDELAY);
        {
            u32x4 a0, a1, a2, a3, b0, b1, b2, b3;
            const unsigned qo = WS_Q + g * 8192 + gq * 512 + c16 * 16;
#pragma unroll 1
            for (int it = 0; it < GCAP; ++it) {
                asm volatile("global_load_dwordx4 %0, %8, %10 sc1\n\tglobal_load_dwordx4 %4, %8, %10 offset:256 sc1\n\t"
                             "global_load_dwordx4 %1, %8, %10 offset:2048 sc1\n\tglobal_load_dwordx4 %5, %8, %10 offset:2304 sc1\n\t"
                             "global_load_dwordx4 %2, %9, %10 sc1\n\tglobal_load_dwordx4 %6, %9, %10 offset:256 sc1\n\t"
                             "global_load_dwordx4 %3, %9, %10 offset:2048 sc1\n\tglobal_load_dwordx4 %7, %9, %10 offset:2304 sc1\n\ts_waitcnt vmcnt(0)"
                             : "=&v"(a0), "=&v"(a1), "=&v"(a2), "=&v"(a3), "=&v"(b0), "=&v"(b1), "=&v"(b2), "=&v"(b3) : "v"(qo), "v"(qo + 4096), "s"(ws) : "memory");
                auto dl = [&](const u32x4& v) { return (v[0] ^ tagl) | (v[2] ^ tagl); };
                auto dh = [&](const u32x4& v) { return (v[1] ^ tagh) | (v[3] ^ tagh); };
                const unsigned bad = ((dl(a0) | dl(a1) | dl(a2) | dl(a3) | dl(b0) | dl(b1) | dl(b2) | dl(b3)) & 0xffu) | dh(a0) | dh(a1) | dh(a2) | dh(a3) | dh(b0) | dh(b1) | dh(b2) | dh(b3);
                if (__all(bad == 0u)) break;
                __builtin_amdgcn_s_sleep(QSLEEP);
            }
            if (s == 0 && lane == 0) st8_sc1(ws, WS_RDY + g * 128, u32x2{tagl, tagh});
            auto val = [&](unsigned v) { return __uint_as_float(v & 0xffffff00u); };
            auto put = [&](const u32x4& va, const u32x4& vb, int i) { const int n = 128 * i + 2 * lane;
                *(LAS unsigned*)(size_t)(ldsb + L_Q + (n >> 8) * 576 + (n & 255) * 2) = cvtpk(val(va[0]) + val(vb[0]), val(va[2]) + val(vb[2])); };
            put(a0, b0, 0); put(a1, b1, 1); put(a2, b2, 2); put(a3, b3, 3);
            asm volatile("s_waitcnt lgkmcnt(0)" ::: "memory");
            if (lane == 0) flag[F_QRDY] = 1u;
        }
        STAMP(2);
        const int hh = s >> 4, db = s & 15;
        lds_wait_ge(flag + F_PCNT, 8, true);
        __builtin_amdgcn_s_sleep(RDELAY);
        {
            u32x2 r; const unsigned ro = WS_RDY + (lane & 7) * 128;
#pragma unroll 1
            for (int it = 0; it < GCAP; ++it) {
                asm volatile("global_load_dwordx2 %0, %1, %2 sc1\n\ts_waitcnt vmcnt(0)" : "=&v"(r) : "v"(ro), "s"(ws) : "memory");
                if (__all((r[0] == tagl) & (r[1] == tagh))) break;
                __builtin_amdgcn_s_sleep(QSLEEP);
            }
        }
        STAMP(4);
        lds_wait_ge(flag + F_LAND, 135, true); lds_wait_ge(flag + F_LAND + 1, 135, true);
        u32x4 wb[14];
        {
            const unsigned wa = ldsb + L_WO + (gq >> 1) * 512 + (8 * (gq & 1) + (c16 >> 2)) * 32 + 8 * (lane & 3);
#pragma unroll
            for (int u = 0; u < 14; ++u) wb[u] = cat2(trread(wa + u * 1024), trread(wa + u * 1024 + 128));
#pragma unroll
            for (int u = 0; u < 14; ++u) asm volatile("" : "+v"(wb[u]));
        }
        STAMP(5);
        lds_wait_ge(flag + F_PUB, 8, false);
        STAMP(3);
        float acc4[4];
        {
            const int d0 = 4 * (((gq & 1) << 1) | (gq >> 1));
            const unsigned pl = WS_PART + (((g * 32 + c16) * 2 + hh) * 16 + db) * 128, po = pl + d0 * 4;
            u32x4 a, b; u32x2 sa, sb;
#pragma unroll 1
            for (int it = 0; it < GCAP; ++it) {
                asm volatile("global_load_dwordx4 %0, %4, %8 sc1\n\tglobal_load_dwordx4 %1, %5, %8 sc1\n\tglobal_load_dwordx2 %2, %6, %8 offset:64 sc1\n\tglobal_load_dwordx2 %3, %7, %8 offset:64 sc1\n\ts_waitcnt vmcnt(0)"
                             : "=&v"(a), "=&v"(b), "=&v"(sa), "=&v"(sb) : "v"(po), "v"(po + 16 * 4096), "v"(pl), "v"(pl + 16 * 4096), "s"(ws) : "memory");
                const unsigned mxa = max(max(max(a[0], a[1]), max(a[2], a[3])), max(sa[0], sa[1])), mxb = max(max(max(b[0], b[1]), max(b[2], b[3])), max(sb[0], sb[1]));
                if (__all(max(mxa, mxb) != SENT)) break;
            }
            STAMP(7);
            const float m0 = __uint_as_float(sa[0]), m1 = __uint_as_float(sb[0]);
            const float M = row_max(fmaxf(m0, m1));
            const float w0 = __builtin_amdgcn_exp2f(m0 - M), w1 = __builtin_amdgcn_exp2f(m1 - M);
            const float inv = 1.0f / row_sum(w0 * __uint_as_float(sa[1]) + w1 * __uint_as_float(sb[1]));
#pragma unroll
            for (int r = 0; r < 4; ++r) acc4[r] = row_sum(fmaf(w0, __uint_as_float(a[r]), w1 * __uint_as_float(b[r]))) * inv;
        }
        {
            const unsigned x0 = cvtpk(acc4[0], acc4[1]), x1 = cvtpk(acc4[2], acc4[3]);
            auto p0 = __builtin_amdgcn_permlane32_swap(x0, x0, false, false);
            auto p1 = __builtin_amdgcn_permlane32_swap(x1, x1, false, false);
            const u32x4 A = {p0[0], p1[0], p0[1], p1[1]};
            const int rg = c16 >> 2, bp = gq >> 1;
            const u32x4 Z = {0, 0, 0, 0};
            const u32x4 A0 = (rg == bp) ? A : Z, A1 = (rg == 2 + bp) ? A : Z;
            f32x4 D[7];
#pragma unroll
            for (int n = 0; n < 7; ++n) D[n] = mfma32(A0, wb[2 * n], f32x4{0, 0, 0, 0});
#pragma unroll
            for (int n = 0; n < 7; ++n) D[n] = mfma32(A1, wb[2 * n + 1], D[n]);
#pragma unroll
            for (int n = 0; n < 7; ++n) __hip_atomic_store((gu32*)(ws + WS_OUTP + blk * (DM * 4)) + 64 * n + lane, __float_as_uint(D[n][0]), RLX_AGENT);
        }
        STAMP(8);
        const int lg = g + 8 * (s >> 3);
        if (s < 16 && lg < 14) {
            const int cb = 32 * lg + 4 * (s & 7);
            const unsigned oo = WS_OUTP + lane * (DM * 4) + cb * 4;
            u32x4 a, b, cc, d;
#pragma unroll 1
            for (int it = 0; it < GCAP; ++it) {
                asm volatile("global_load_dwordx4 %0, %4, %8 sc1\n\tglobal_load_dwordx4 %1, %5, %8 sc1\n\tglobal_load_dwordx4 %2, %6, %8 sc1\n\tglobal_load_dwordx4 %3, %7, %8 sc1\n\ts_waitcnt vmcnt(0)"
                             : "=&v"(a), "=&v"(b), "=&v"(cc), "=&v"(d) : "v"(oo), "v"(oo + 64 * DM * 4), "v"(oo + 128 * DM * 4), "v"(oo + 192 * DM * 4), "s"(ws) : "memory");
                bool ok = true;
#pragma unroll
                for (int r = 0; r < 4; ++r) ok = ok & (a[r] != SENT) & (b[r] != SENT) & (cc[r] != SENT) & (d[r] != SENT);
                if (__all(ok)) break;
            }
            STAMP(9);
            float v[4];
#pragma unroll
            for (int r = 0; r < 4; ++r) v[r] = (__uint_as_float(a[r]) + __uint_as_float(b[r])) + (__uint_as_float(cc[r]) + __uint_as_float(d[r]));
            const float y0 = swap32_add(v[0], v[1]), y1 = swap32_add(v[2], v[3]);
            const float z = row_sum(swap16_add(y0, y1));
            const unsigned pk2 = cvtpk(z, z);
            if (c16 == 0) out[cb + 2 * (gq & 1) + (gq >> 1)] = (unsigned short)(pk2 & 0xffffu);
            if (blockIdx.x == 0 && lane == 0) __hip_atomic_store((gu64*)(ws + WS_E), E + 1ull, RLX_AGENT);
            STAMP(10);
        }
    } else {
        const int c = wave - 3;
        asm volatile("s_barrier" ::: "memory");
        lds_wait_ge(flag + F_LAND + (c & 1), 16 * ((c >> 1) + 1), true);
        STAMP(1);
        u32x4 kf[16];
        {
            const int r7 = c16 & 7;
            const unsigned kb0 = ldsb + L_SLOT + c * 16384 + (c16 >> 3) * 4096 + r7 * 128 + ((gq ^ r7) * 16), kb1 = kb0 ^ 64;
#pragma unroll
            for (int t = 0; t < 2; ++t)
#pragma unroll
                for (int ds = 0; ds < 8; ++ds)
                    kf[t * 8 + ds] = *(LAS u32x4*)(size_t)(((ds & 1) ? kb1 : kb0) + (ds >> 1) * 1024 + t * 8192);
#pragma unroll
            for (int i = 0; i < 16; ++i) asm volatile("" : "+v"(kf[i]));
            asm volatile("" ::: "memory");
            if (lane == 0) flag[F_KREL + c] = 1u;
        }
        lds_wait_ge(flag + F_QRDY, 1, true);
        STAMP(2);
        const int hd = (c16 >> 2) & 1;
        f32x4 s0 = {0, 0, 0, 0}, s1 = {0, 0, 0, 0};
        {
            const unsigned qa = ldsb + L_Q + hd * 576 + gq * 16;
            u32x4 qf[8];
#pragma unroll
            for (int ds = 0; ds < 8; ++ds) qf[ds] = *(LAS u32x4*)(size_t)(qa + ds * 64);
#pragma unroll
            for (int ds = 0; ds < 8; ++ds) { s0 = mfma32(kf[ds], qf[ds], s0); s1 = mfma32(kf[8 + ds], qf[ds], s1); }
        }
        constexpr float C2 = 0.0625f * 1.4426950408889634f;
        float sv[8] = {s0[0] * C2, s0[1] * C2, s0[2] * C2, s0[3] * C2, s1[0] * C2, s1[1] * C2, s1[2] * C2, s1[3] * C2};
        float mx = fmaxf(fmaxf(fmaxf(sv[0], sv[1]), fmaxf(sv[2], sv[3])), fmaxf(fmaxf(sv[4], sv[5]), fmaxf(sv[6], sv[7])));
        mx = swap32_max(swap16_max(mx));
        float p[8], ls = 0.f;
#pragma unroll
        for (int i = 0; i < 8; ++i) { p[i] = __builtin_amdgcn_exp2f(sv[i] - mx); ls += p[i]; }
        ls = swap16_add(ls, ls); ls = swap32_add(ls, ls);
        {
            const u32x4 pf = {cvtpk(p[0], p[1]), cvtpk(p[2], p[3]), cvtpk(p[4], p[5]), cvtpk(p[6], p[7])};
            *(LAS u32x4*)(size_t)(ldsb + L_P + c * 1024 + lane * 16) = pf;
            if (lane == 0 || lane == 4) {
                ((volatile LAS float*)(size_t)(ldsb + L_M))[hd * 8 + c] = mx;
                ((volatile LAS float*)(size_t)(ldsb + L_L))[hd * 8 + c] = ls;
            }
            asm volatile("s_waitcnt lgkmcnt(0)" ::: "memory");
            if (lane == 0) __hip_atomic_fetch_add((LAS unsigned*)(flag + F_PCNT), 1u, __ATOMIC_RELAXED, __HIP_MEMORY_SCOPE_WORKGROUP);
        }
        STAMP(3);
        lds_wait_ge(flag + F_PCNT, 8, false);
        f32x4 o0 = {0, 0, 0, 0}, o1 = {0, 0, 0, 0};
        float M, Lw;
        {
            const f32x4 ma = *(LAS f32x4*)(size_t)(ldsb + L_M + hd * 32), mb = *(LAS f32x4*)(size_t)(ldsb + L_M + hd * 32 + 16);
            const f32x4 la = *(LAS f32x4*)(size_t)(ldsb + L_L + hd * 32), lb = *(LAS f32x4*)(size_t)(ldsb + L_L + hd * 32 + 16);
            M = fmaxf(fmaxf(fmaxf(ma[0], ma[1]), fmaxf(ma[2], ma[3])), fmaxf(fmaxf(mb[0], mb[1]), fmaxf(mb[2], mb[3])));
            Lw = 0.f;
#pragma unroll
            for (int i = 0; i < 4; ++i) { Lw = fmaf(la[i], __builtin_amdgcn_exp2f(ma[i] - M), Lw); Lw = fmaf(lb[i], __builtin_amdgcn_exp2f(mb[i] - M), Lw); }
            const int r7 = 4 * (gq & 1) + (c16 >> 2), pp = lane & 3;
            unsigned ab = ldsb + L_SLOT + ((gq >> 1) * 4 + (c >> 1)) * 1024 + r7 * 128 + (((4 * (c & 1) + (pp >> 1)) ^ r7) * 16) + 8 * (pp & 1);
            unsigned pad = ldsb + L_P + lane * 16, mad = ldsb + L_M + hd * 32;
#pragma unroll 1
            for (int ks = 0; ks < 8; ++ks) {
                const u32x4 pa = *(LAS u32x4*)(size_t)pad;
                const float f = __builtin_amdgcn_exp2f(*(LAS float*)(size_t)mad - M);
                lds_wait_ge(flag + F_LAND + (ks & 1), 80 + 16 * (ks >> 1), false);
                const u32x4 b0 = cat2(trread(ab), trread(ab + 8192)), b1 = cat2(trread(ab ^ 32), trread((ab ^ 32) + 8192));
                const f32x4 d0 = mfma32(b0, pa, f32x4{0, 0, 0, 0}), d1 = mfma32(b1, pa, f32x4{0, 0, 0, 0});
#pragma unroll
                for (int r = 0; r < 4; ++r) { o0[r] = fmaf(f, d0[r], o0[r]); o1[r] = fmaf(f, d1[r], o1[r]); }
                ab += 16384; pad += 1024; mad += 4;
            }
        }
        STAMP(4);
        {
            const int cp = c16 >> 2, dt = cp >> 1;
            const unsigned ln = WS_PART + ((blk * 2 + (cp & 1)) * 16 + 2 * c + dt) * 128;
            const f32x4 ov = dt ? o1 : o0;
            if ((lane & 3) == 0) st16_sc1(ws, ln + gq * 16, __builtin_bit_cast(u32x4, ov));
            if ((lane & 3) == 1 && gq == 0) st8_sc1(ws, ln + 64, u32x2{__float_as_uint(M), __float_as_uint(Lw)});
            STAMP(5);
            asm volatile("s_waitcnt vmcnt(0)" ::: "memory");
            if (lane == 0) __hip_atomic_fetch_add((LAS unsigned*)(flag + F_PUB), 1u, __ATOMIC_RELAXED, __HIP_MEMORY_SCOPE_WORKGROUP);
        }
        STAMP(6);
    }
    STAMP_FLUSH();
}

extern "C" void kernel_launch(void* const* d_in, const int* in_sizes, int n_in, void* d_out, int out_size, void* d_ws, size_t ws_size, hipStream_t stream) {
    (void)hipFuncSetAttribute((const void*)fused_decode, hipFuncAttributeMaxDynamicSharedMemorySize, LDS_ALLOC);
    fused_decode<<<256, NTHR, LDS_ALLOC, stream>>>((const unsigned short*)d_in[0], (const unsigned short*)d_in[1], (const unsigned short*)d_in[2],
                                                   (const unsigned short*)d_in[3], (const unsigned short*)d_in[4], (unsigned short*)d_out, (unsigned char*)d_ws);
}
```

```cpp
#include <hip/hip_runtime.h>
#include <stdint.h>

typedef unsigned u32x4 __attribute__((ext_vector_type(4)));
typedef unsigned u32x2 __attribute__((ext_vector_type(2)));
typedef float f32x4 __attribute__((ext_vector_type(4)));
typedef __bf16 bf16x8 __attribute__((ext_vector_type(8)));
typedef short s16x4 __attribute__((ext_vector_type(4)));
#define GAS __attribute__((address_space(1)))
#define LAS __attribute__((address_space(3)))
typedef GAS unsigned long long gu64;
typedef GAS unsigned gu32;
#define RLX_AGENT __ATOMIC_RELAXED, __HIP_MEMORY_SCOPE_AGENT

#define STAMPS 1
#define DEVPRINT 1
#include <cstdio>
#define WIN 16
#define QSLEEP 16
#define RDELAY 16
#define QDELAY 8
#define KV_NT 1

constexpr int DM = 448, SK = 8192, NQ = 4096;
constexpr int NWAVE = 11, NTHR = NWAVE * 64;
constexpr int WS_E = 0, WS_RDY = 4096, WS_Q = 8192, WS_PART = WS_Q + 65536, WS_OUTP = WS_PART + 1048576;
constexpr int WS_END = WS_OUTP + 256 * DM * 4;
constexpr size_t STAMP_OFF = 4u << 20;
constexpr int L_SLOT = 0, L_WO = 131072, L_P = L_WO + 14336, L_Q = L_P + 8192, L_X = L_Q + 1152, L_M = L_X + 1024, L_L = L_M + 64, L_FLAG = L_L + 64, LDS_TOTAL = L_FLAG + 256;
constexpr int F_LAND = 0, F_KREL = 16, F_QRDY = 26, F_PCNT = 27, F_PUB = 28, F_NUM = 32;
constexpr unsigned SENT = 0xFFFFFFFFu;
constexpr int GCAP = 1 << 15, LCAP = 1 << 20;

__device__ __forceinline__ float bf2f(unsigned b) { return __builtin_bit_cast(float, b << 16); }
typedef __bf16 bf16x2 __attribute__((ext_vector_type(2)));
__device__ __forceinline__ unsigned cvtpk(float lo, float hi) { bf16x2 v = {(__bf16)lo, (__bf16)hi}; return __builtin_bit_cast(unsigned, v); }
__device__ __forceinline__ f32x4 mfma32(u32x4 a, u32x4 b, f32x4 c) {
    return __builtin_amdgcn_mfma_f32_16x16x32_bf16(__builtin_bit_cast(bf16x8, a), __builtin_bit_cast(bf16x8, b), c, 0, 0, 0);
}
template <int CTRL> __device__ __forceinline__ float dpp(float x) {
    return __builtin_bit_cast(float, __builtin_amdgcn_mov_dpp(__builtin_bit_cast(int, x), CTRL, 0xf, 0xf, true));
}
constexpr int XOR1 = 0xB1, XOR2 = 0x4E, HMIR = 0x141, MIR = 0x140, ROR4 = 0x124, ROR8 = 0x128;
__device__ __forceinline__ float row_sum(float x) { x += dpp<XOR1>(x); x += dpp<XOR2>(x); x += dpp<HMIR>(x); x += dpp<MIR>(x); return x; }
__device__ __forceinline__ float row_max(float x) { x = fmaxf(x, dpp<XOR1>(x)); x = fmaxf(x, dpp<XOR2>(x)); x = fmaxf(x, dpp<HMIR>(x)); x = fmaxf(x, dpp<MIR>(x)); return x; }
__device__ __forceinline__ float swap32_add(float a, float b) { auto t = __builtin_amdgcn_permlane32_swap(__float_as_uint(a), __float_as_uint(b), false, false); return __uint_as_float(t[0]) + __uint_as_float(t[1]); }
__device__ __forceinline__ float swap16_add(float a, float b) { auto t = __builtin_amdgcn_permlane16_swap(__float_as_uint(a), __float_as_uint(b), false, false); return __uint_as_float(t[0]) + __uint_as_float(t[1]); }
__device__ __forceinline__ float swap32_max(float a) { auto t = __builtin_amdgcn_permlane32_swap(__float_as_uint(a), __float_as_uint(a), false, false); return fmaxf(__uint_as_float(t[0]), __uint_as_float(t[1])); }
__device__ __forceinline__ float swap16_max(float a) { auto t = __builtin_amdgcn_permlane16_swap(__float_as_uint(a), __float_as_uint(a), false, false); return fmaxf(__uint_as_float(t[0]), __uint_as_float(t[1])); }

template <int NT> __device__ __forceinline__ void dma(unsigned voff, const void* sbase, unsigned m0v) {
    if (NT) asm volatile("s_mov_b32 m0, %2\n\ts_nop 0\n\tglobal_load_lds_dwordx4 %0, %1 nt" :: "v"(voff), "s"(sbase), "s"(m0v) : "memory");
    else asm volatile("s_mov_b32 m0, %2\n\ts_nop 0\n\tglobal_load_lds_dwordx4 %0, %1" :: "v"(voff), "s"(sbase), "s"(m0v) : "memory");
}
template <int N> __device__ __forceinline__ void vmwait() { asm volatile("s_waitcnt vmcnt(%0)" :: "i"(N) : "memory"); }
__device__ __forceinline__ void lds_wait_ge(volatile LAS unsigned* p, int want, bool slp) {
#pragma unroll 1
    for (int it = 0; it < LCAP; ++it) { if (__builtin_amdgcn_readfirstlane((int)*p) >= want) break; if (slp) __builtin_amdgcn_s_sleep(1); }
    asm volatile("" ::: "memory");
}
__device__ __forceinline__ void st16_sc1(const void* sbase, unsigned voff, u32x4 v) { asm volatile("global_store_dwordx4 %0, %1, %2 sc1\n\ts_nop 1" :: "v"(voff), "v"(v), "s"(sbase) : "memory"); }
__device__ __forceinline__ void st8_sc1(const void* sbase, unsigned voff, u32x2 v) { asm volatile("global_store_dwordx2 %0, %1, %2 sc1" :: "v"(voff), "v"(v), "s"(sbase) : "memory"); }
__device__ __forceinline__ void st4_sc1(const void* sbase, unsigned voff, unsigned v) { asm volatile("global_store_dword %0, %1, %2 sc1" :: "v"(voff), "v"(v), "s"(sbase) : "memory"); }
__device__ __forceinline__ s16x4 trread(unsigned addr) { return __builtin_amdgcn_ds_read_tr16_b64_v4i16((LAS s16x4*)(size_t)addr); }
__device__ __forceinline__ u32x4 cat2(s16x4 a, s16x4 b) { u32x2 x = __builtin_bit_cast(u32x2, a), y = __builtin_bit_cast(u32x2, b); return u32x4{x[0], x[1], y[0], y[1]}; }

constexpr int L_STAMP = LDS_TOTAL;
#define LDS_ALLOC (LDS_TOTAL + NWAVE * 128)
#define STAMP(i) do { const unsigned long long t_ = __builtin_amdgcn_s_memrealtime(); if (lane == 0) *(volatile LAS unsigned long long*)(size_t)(ldsb + L_STAMP + wave * 128 + (i) * 8) = t_; } while (0)
#define STAMP_FLUSH() do { asm volatile("s_waitcnt lgkmcnt(0)" ::: "memory"); if (lane < 16) ((gu64*)((char*)ws + STAMP_OFF))[(blockIdx.x * NWAVE + wave) * 16 + lane] = *(volatile LAS unsigned long long*)(size_t)(ldsb + L_STAMP + wave * 128 + lane * 8); } while (0)
#define STAMP_INIT() do { if (lane < 16) *(volatile LAS unsigned long long*)(size_t)(ldsb + L_STAMP + wave * 128 + lane * 8) = 0ull; } while (0)

#define STR_(x) #x
#define XSTR(x) STR_(x)
#define KVNT " nt"

__global__ __launch_bounds__(NTHR) void fused_decode(const unsigned short* __restrict__ x, const unsigned short* __restrict__ Kc, const unsigned short* __restrict__ Vc,
                                                     const unsigned short* __restrict__ wq, const unsigned short* __restrict__ wo, unsigned short* __restrict__ out, unsigned char* ws) {
    extern __shared__ __attribute__((aligned(16))) unsigned char smem[];
    const int tid = threadIdx.x, lane = tid & 63, wave = __builtin_amdgcn_readfirstlane(tid >> 6);
    const int g = blockIdx.x & 7, s = blockIdx.x >> 3, blk = g * 32 + s;
    const unsigned ldsb = (unsigned)(size_t)(LAS unsigned char*)smem;
    volatile LAS unsigned* flag = (volatile LAS unsigned*)(size_t)(ldsb + L_FLAG);
    const int gq = lane >> 4, c16 = lane & 15;
    STAMP_INIT();
    STAMP(0);
    const unsigned long long clk0 = __builtin_amdgcn_s_memtime(), rt0 = __builtin_amdgcn_s_memrealtime();
    if (wave < 2) {
        const int L = wave, hi = lane >> 5, l31 = lane & 31;
        const char* Kb = (const char*)Kc + (size_t)blk * 131072 + L * 16384;
        const char* Vb = (const char*)Vc + (size_t)blk * 131072 + L * 16384;
        const int hh = s >> 4, db = s & 15;
        const char* Wb = (const char*)wo + (size_t)((2 * g + hh) * 256 + 16 * db) * (DM * 2) + L * 64;
        const unsigned voff = (lane >> 3) * 512 + (((lane & 7) ^ (lane >> 3)) * 16);
        const unsigned wof = (l31 >> 1) * (DM * 2) + hi * 32 + (l31 & 1) * 16;
        const unsigned sl = ldsb + L_SLOT + L * 16384;
        const unsigned vland = ldsb + L_FLAG + (F_LAND + L) * 4, vrel = ldsb + L_FLAG + (F_KREL + L) * 4;
        asm volatile(
            "s_mov_b32 s40, 0\n\t"
            "s_mov_b64 s[42:43], %[kb]\n\t"
            "s_mov_b32 s44, 0-" XSTR(WIN) "\n\t"
            "v_mov_b32 v20, %[vrel]\n\t"
            "v_mov_b32 v21, %[vland]\n\t"
            "1:\n\t"
            "s_mov_b32 s45, %[sl]\n\t"
            "s_mov_b32 s41, 4\n\t"
            "2:\n\t"
            "s_cmp_eq_u32 s40, 0\n\t"
            "s_cbranch_scc1 4f\n\t"
            "s_mov_b32 s46, 0x100000\n\t"
            "3:\n\t"
            "ds_read_b32 v22, v20\n\t"
            "s_waitcnt lgkmcnt(0)\n\t"
            "v_readfirstlane_b32 s47, v22\n\t"
            "s_sub_u32 s46, s46, 1\n\t"
            "s_cbranch_scc1 5f\n\t"
            "s_cmp_eq_u32 s47, 0\n\t"
            "s_cbranch_scc1 3b\n\t"
            "5:\n\t"
            "v_add_u32 v20, 8, v20\n\t"
            "4:\n\t"
            "s_mov_b32 s46, 4\n\t"
            "6:\n\t"
            "s_mov_b32 m0, s45\n\t"
            "s_add_u32 s45, s45, 4096\n\t"
            "global_load_lds_dwordx4 %[voff], s[42:43]" KVNT "\n\t"
            "s_addk_i32 m0, 896\n\t"
            "s_waitcnt vmcnt(" XSTR(WIN) ")\n\t"
            "global_load_lds_dwordx4 %[voff], s[42:43] offset:128" KVNT "\n\t"
            "s_addk_i32 m0, 896\n\t"
            "s_waitcnt vmcnt(" XSTR(WIN) ")\n\t"
            "global_load_lds_dwordx4 %[voff], s[42:43] offset:256" KVNT "\n\t"
            "s_addk_i32 m0, 896\n\t"
            "s_waitcnt vmcnt(" XSTR(WIN) ")\n\t"
            "global_load_lds_dwordx4 %[voff], s[42:43] offset:384" KVNT "\n\t"
            "s_add_u32 s42, s42, 4096\n\t"
            "s_addc_u32 s43, s43, 0\n\t"
            "s_add_i32 s44, s44, 4\n\t"
            "v_mov_b32 v22, s44\n\t"
            "s_waitcnt vmcnt(" XSTR(WIN) ")\n\t"
            "ds_write_b32 v21, v22\n\t"
            "s_cmp_lg_u32 s44, 0\n\t"
            "s_cbranch_scc1 7f\n\t"
            "s_barrier\n\t"
            "ds_write_b32 v21, v22\n\t"
            "7:\n\t"
            "s_sub_u32 s46, s46, 1\n\t"
            "s_cmp_lg_u32 s46, 0\n\t"
            "s_cbranch_scc1 6b\n\t"
            "s_add_u32 s42, s42, 16384\n\t"
            "s_addc_u32 s43, s43, 0\n\t"
            "s_add_u32 s45, s45, 16384\n\t"
            "s_sub_u32 s41, s41, 1\n\t"
            "s_cmp_lg_u32 s41, 0\n\t"
            "s_cbranch_scc1 2b\n\t"
            "s_mov_b64 s[42:43], %[vb]\n\t"
            "s_add_u32 s40, s40, 1\n\t"
            "s_cmp_eq_u32 s40, 1\n\t"
            "s_cbranch_scc1 1b\n\t"
            "s_mov_b64 s[42:43], %[wb]\n\t"
            "s_mov_b32 m0, %[lwo]\n\t"
            "s_mov_b32 s46, 7\n\t"
            "8:\n\t"
            "global_load_lds_dwordx4 %[wof], s[42:43]\n\t"
            "s_addk_i32 m0, 2048\n\t"
            "s_add_u32 s42, s42, 128\n\t"
            "s_addc_u32 s43, s43, 0\n\t"
            "s_sub_u32 s46, s46, 1\n\t"
            "s_cmp_lg_u32 s46, 0\n\t"
            "s_cbranch_scc1 8b\n\t"
            ".if 128-" XSTR(WIN) " < 80\n\ts_waitcnt vmcnt(55)\n\tv_mov_b32 v22, 80\n\tds_write_b32 v21, v22\n\t.endif\n\t"
            ".if 128-" XSTR(WIN) " < 96\n\ts_waitcnt vmcnt(39)\n\tv_mov_b32 v22, 96\n\tds_write_b32 v21, v22\n\t.endif\n\t"
            ".if 128-" XSTR(WIN) " < 112\n\ts_waitcnt vmcnt(23)\n\tv_mov_b32 v22, 112\n\tds_write_b32 v21, v22\n\t.endif\n\t"
            "s_waitcnt vmcnt(7)\n\tv_mov_b32 v22, 128\n\tds_write_b32 v21, v22\n\t"
            "s_waitcnt vmcnt(0)\n\tv_mov_b32 v22, 135\n\tds_write_b32 v21, v22\n\t"
            "s_waitcnt lgkmcnt(0)\n\t"
            :: [kb] "s"(Kb), [vb] "s"(Vb), [wb] "s"(Wb), [sl] "s"(sl), [lwo] "s"(ldsb + L_WO + L * 1024), [voff] "v"(voff), [wof] "v"(wof), [vrel] "s"(vrel), [vland] "s"(vland)
            : "s40", "s41", "s42", "s43", "s44", "s45", "s46", "s47", "v20", "v21", "v22", "scc", "memory");
        STAMP(3);
    } else if (wave == 2) {
        if (lane < F_NUM) flag[lane] = 0u;
        const unsigned long long E = __hip_atomic_load((gu64*)(ws + WS_E), RLX_AGENT);
        {
            const int cbk = s >> 1, rh = s & 1;
            const char* wqb = (const char*)wq + (size_t)(224 * rh) * (NQ * 2) + (512 * g + 32 * cbk) * 2;
            const unsigned wqo = (lane >> 2) * (NQ * 2) + (lane & 3) * 16;
#pragma unroll 1
            for (int i = 0; i < 14; ++i) dma<0>(wqo, wqb + (size_t)i * 16 * (NQ * 2), ldsb + L_WO + i * 1024);
            if (lane < 28) dma<0>(lane * 16, (const char*)x + rh * 448, ldsb + L_X);
        }
        {
            const u32x4 sv = {SENT, SENT, SENT, SENT};
#pragma unroll
            for (int i = 0; i < 4; ++i) st16_sc1(ws, WS_PART + blk * 4096 + i * 1024 + lane * 16, sv);
            st16_sc1(ws, WS_OUTP + blk * (DM * 4) + lane * 16, sv);
            if (lane < 48) st16_sc1(ws, WS_OUTP + blk * (DM * 4) + 1024 + lane * 16, sv);
        }
        asm volatile("s_waitcnt lgkmcnt(0)\n\ts_barrier" ::: "memory");
        asm volatile("s_waitcnt vmcnt(0)" ::: "memory");
        STAMP(1);
        float qv;
        {
            const unsigned wa = ldsb + L_WO + (8 * gq + (c16 >> 2)) * 64 + 8 * (lane & 3), xa = ldsb + L_X + 16 * gq;
            u32x4 bw[14], ax[7];
#pragma unroll
            for (int ks = 0; ks < 7; ++ks) {
                bw[2 * ks] = cat2(trread(wa + ks * 2048), trread(wa + ks * 2048 + 256));
                bw[2 * ks + 1] = cat2(trread(wa + ks * 2048 + 32), trread(wa + ks * 2048 + 256 + 32));
                ax[ks] = *(LAS u32x4*)(size_t)(xa + ks * 64);
            }
            f32x4 d0 = {0, 0, 0, 0}, d1 = {0, 0, 0, 0};
#pragma unroll
            for (int ks = 0; ks < 7; ++ks) { d0 = mfma32(ax[ks], bw[2 * ks], d0); d1 = mfma32(ax[ks], bw[2 * ks + 1], d1); }
            qv = (gq & 1) ? d1[0] : d0[0];
        }
        const unsigned long long tag = E + 1ull;
        const unsigned tagl = (unsigned)tag & 0xffu, tagh = (unsigned)(tag >> 8);
        if (lane < 32) st8_sc1(ws, WS_Q + g * 8192 + s * 256 + lane * 8, u32x2{((__float_as_uint(qv) + 0x80u) & 0xffffff00u) | tagl, tagh});
        asm volatile("s_waitcnt vmcnt(0)" ::: "memory");
        STAMP(6);
        __builtin_amdgcn_s_sleep(QDELAY);
        {
            u32x4 a0, a1, a2, a3, b0, b1, b2, b3;
            const unsigned qo = WS_Q + g * 8192 + gq * 512 + c16 * 16;
#pragma unroll 1
            for (int it = 0; it < GCAP; ++it) {
                asm volatile("global_load_dwordx4 %0, %8, %10 sc1\n\tglobal_load_dwordx4 %4, %8, %10 offset:256 sc1\n\t"
                             "global_load_dwordx4 %1, %8, %10 offset:2048 sc1\n\tglobal_load_dwordx4 %5, %8, %10 offset:2304 sc1\n\t"
                             "global_load_dwordx4 %2, %9, %10 sc1\n\tglobal_load_dwordx4 %6, %9, %10 offset:256 sc1\n\t"
                             "global_load_dwordx4 %3, %9, %10 offset:2048 sc1\n\tglobal_load_dwordx4 %7, %9, %10 offset:2304 sc1\n\ts_waitcnt vmcnt(0)"
                             : "=&v"(a0), "=&v"(a1), "=&v"(a2), "=&v"(a3), "=&v"(b0), "=&v"(b1), "=&v"(b2), "=&v"(b3) : "v"(qo), "v"(qo + 4096), "s"(ws) : "memory");
                auto dl = [&](const u32x4& v) { return (v[0] ^ tagl) | (v[2] ^ tagl); };
                auto dh = [&](const u32x4& v) { return (v[1] ^ tagh) | (v[3] ^ tagh); };
                const unsigned bad = ((dl(a0) | dl(a1) | dl(a2) | dl(a3) | dl(b0) | dl(b1) | dl(b2) | dl(b3)) & 0xffu) | dh(a0) | dh(a1) | dh(a2) | dh(a3) | dh(b0) | dh(b1) | dh(b2) | dh(b3);
                if (__all(bad == 0u)) break;
                __builtin_amdgcn_s_sleep(QSLEEP);
            }
            if (s == 0 && lane == 0) st8_sc1(ws, WS_RDY + g * 128, u32x2{tagl, tagh});
            auto val = [&](unsigned v) { return __uint_as_float(v & 0xffffff00u); };
            auto put = [&](const u32x4& va, const u32x4& vb, int i) { const int n = 128 * i + 2 * lane;
                *(LAS unsigned*)(size_t)(ldsb + L_Q + (n >> 8) * 576 + (n & 255) * 2) = cvtpk(val(va[0]) + val(vb[0]), val(va[2]) + val(vb[2])); };
            put(a0, b0, 0); put(a1, b1, 1); put(a2, b2, 2); put(a3, b3, 3);
            asm volatile("s_waitcnt lgkmcnt(0)" ::: "memory");
            if (lane == 0) flag[F_QRDY] = 1u;
        }
        STAMP(2);
        const int hh = s >> 4, db = s & 15;
        lds_wait_ge(flag + F_PCNT, 8, true);
        __builtin_amdgcn_s_sleep(RDELAY);
        {
            u32x2 r; const unsigned ro = WS_RDY + (lane & 7) * 128;
#pragma unroll 1
            for (int it = 0; it < GCAP; ++it) {
                asm volatile("global_load_dwordx2 %0, %1, %2 sc1\n\ts_waitcnt vmcnt(0)" : "=&v"(r) : "v"(ro), "s"(ws) : "memory");
                if (__all((r[0] == tagl) & (r[1] == tagh))) break;
                __builtin_amdgcn_s_sleep(QSLEEP);
            }
        }
        STAMP(4);
        lds_wait_ge(flag + F_LAND, 135, true); lds_wait_ge(flag + F_LAND + 1, 135, true);
        u32x4 wb[14];
        {
            const unsigned wa = ldsb + L_WO + (gq >> 1) * 512 + (8 * (gq & 1) + (c16 >> 2)) * 32 + 8 * (lane & 3);
#pragma unroll
            for (int u = 0; u < 14; ++u) wb[u] = cat2(trread(wa + u * 1024), trread(wa + u * 1024 + 128));
#pragma unroll
            for (int u = 0; u < 14; ++u) asm volatile("" : "+v"(wb[u]));
        }
        STAMP(5);
        lds_wait_ge(flag + F_PUB, 8, false);
        STAMP(3);
        float acc4[4];
        {
            const int d0 = 4 * (((gq & 1) << 1) | (gq >> 1));
            const unsigned pl = WS_PART + (((g * 32 + c16) * 2 + hh) * 16 + db) * 128, po = pl + d0 * 4;
            u32x4 a, b; u32x2 sa, sb;
#pragma unroll 1
            for (int it = 0; it < GCAP; ++it) {
                asm volatile("global_load_dwordx4 %0, %4, %8 sc1\n\tglobal_load_dwordx4 %1, %5, %8 sc1\n\tglobal_load_dwordx2 %2, %6, %8 offset:64 sc1\n\tglobal_load_dwordx2 %3, %7, %8 offset:64 sc1\n\ts_waitcnt vmcnt(0)"
                             : "=&v"(a), "=&v"(b), "=&v"(sa), "=&v"(sb) : "v"(po), "v"(po + 16 * 4096), "v"(pl), "v"(pl + 16 * 4096), "s"(ws) : "memory");
                const unsigned mxa = max(max(max(a[0], a[1]), max(a[2], a[3])), max(sa[0], sa[1])), mxb = max(max(max(b[0], b[1]), max(b[2], b[3])), max(sb[0], sb[1]));
                if (__all(max(mxa, mxb) != SENT)) break;
            }
            STAMP(7);
            const float m0 = __uint_as_float(sa[0]), m1 = __uint_as_float(sb[0]);
            const float M = row_max(fmaxf(m0, m1));
            const float w0 = __builtin_amdgcn_exp2f(m0 - M), w1 = __builtin_amdgcn_exp2f(m1 - M);
            const float inv = 1.0f / row_sum(w0 * __uint_as_float(sa[1]) + w1 * __uint_as_float(sb[1]));
#pragma unroll
            for (int r = 0; r < 4; ++r) acc4[r] = row_sum(fmaf(w0, __uint_as_float(a[r]), w1 * __uint_as_float(b[r]))) * inv;
        }
        {
            const unsigned x0 = cvtpk(acc4[0], acc4[1]), x1 = cvtpk(acc4[2], acc4[3]);
            auto p0 = __builtin_amdgcn_permlane32_swap(x0, x0, false, false);
            auto p1 = __builtin_amdgcn_permlane32_swap(x1, x1, false, false);
            const u32x4 A = {p0[0], p1[0], p0[1], p1[1]};
            const int rg = c16 >> 2, bp = gq >> 1;
            const u32x4 Z = {0, 0, 0, 0};
            const u32x4 A0 = (rg == bp) ? A : Z, A1 = (rg == 2 + bp) ? A : Z;
            f32x4 D[7];
#pragma unroll
            for (int n = 0; n < 7; ++n) D[n] = mfma32(A0, wb[2 * n], f32x4{0, 0, 0, 0});
#pragma unroll
            for (int n = 0; n < 7; ++n) D[n] = mfma32(A1, wb[2 * n + 1], D[n]);
#pragma unroll
            for (int n = 0; n < 7; ++n) __hip_atomic_store((gu32*)(ws + WS_OUTP + blk * (DM * 4)) + 64 * n + lane, __float_as_uint(D[n][0]), RLX_AGENT);
        }
        STAMP(8);
        const int lg = g + 8 * (s >> 3);
        if (s < 16 && lg < 14) {
            const int cb = 32 * lg + 4 * (s & 7);
            const unsigned oo = WS_OUTP + lane * (DM * 4) + cb * 4;
            u32x4 a, b, cc, d;
#pragma unroll 1
            for (int it = 0; it < GCAP; ++it) {
                asm volatile("global_load_dwordx4 %0, %4, %8 sc1\n\tglobal_load_dwordx4 %1, %5, %8 sc1\n\tglobal_load_dwordx4 %2, %6, %8 sc1\n\tglobal_load_dwordx4 %3, %7, %8 sc1\n\ts_waitcnt vmcnt(0)"
                             : "=&v"(a), "=&v"(b), "=&v"(cc), "=&v"(d) : "v"(oo), "v"(oo + 64 * DM * 4), "v"(oo + 128 * DM * 4), "v"(oo + 192 * DM * 4), "s"(ws) : "memory");
                bool ok = true;
#pragma unroll
                for (int r = 0; r < 4; ++r) ok = ok & (a[r] != SENT) & (b[r] != SENT) & (cc[r] != SENT) & (d[r] != SENT);
                if (__all(ok)) break;
            }
            STAMP(9);
            float v[4];
#pragma unroll
            for (int r = 0; r < 4; ++r) v[r] = (__uint_as_float(a[r]) + __uint_as_float(b[r])) + (__uint_as_float(cc[r]) + __uint_as_float(d[r]));
            const float y0 = swap32_add(v[0], v[1]), y1 = swap32_add(v[2], v[3]);
            const float z = row_sum(swap16_add(y0, y1));
            const unsigned pk2 = cvtpk(z, z);
            if (c16 == 0) out[cb + 2 * (gq & 1) + (gq >> 1)] = (unsigned short)(pk2 & 0xffffu);
            if (blockIdx.x == 0 && lane == 0) __hip_atomic_store((gu64*)(ws + WS_E), E + 1ull, RLX_AGENT);
            STAMP(10);
            if ((blockIdx.x == 0 || blockIdx.x == 77) && lane == 0) {
                const unsigned long long clk1 = __builtin_amdgcn_s_memtime(), rt1 = __builtin_amdgcn_s_memrealtime();
                volatile LAS unsigned long long* S = (volatile LAS unsigned long long*)(size_t)(ldsb + L_STAMP);
                const unsigned long long z = S[2 * 16];
                printf("blk %d clk %.0f MHz | aux: wq %d qdr %d qg %d rdy %d wof %d pub %d h1 %d sB %d h2 %d end %d | L0 %d L1 %d | c0: K %d q %d P %d PV %d pub %d | c7: K %d q %d P %d PV %d (x10ns)\n", (int)blockIdx.x,
                       (double)(clk1 - clk0) / (double)(rt1 - rt0) * 100.0,
                       (int)(S[32 + 1] - z), (int)(S[32 + 6] - z), (int)(S[32 + 2] - z), (int)(S[32 + 4] - z), (int)(S[32 + 5] - z), (int)(S[32 + 3] - z), (int)(S[32 + 7] - z), (int)(S[32 + 8] - z), (int)(S[32 + 9] - z), (int)(S[32 + 10] - z),
                       (int)(S[0 + 3] - z), (int)(S[16 + 3] - z), (int)(S[48 + 1] - z), (int)(S[48 + 2] - z), (int)(S[48 + 3] - z), (int)(S[48 + 4] - z), (int)(S[48 + 5] - z),
                       (int)(S[160 + 1] - z), (int)(S[160 + 2] - z), (int)(S[160 + 3] - z), (int)(S[160 + 4] - z));
            }
        }
    } else {
        const int c = wave - 3;
        asm volatile("s_barrier" ::: "memory");
        lds_wait_ge(flag + F_LAND + (c & 1), 16 * ((c >> 1) + 1), true);
        STAMP(1);
        u32x4 kf[16];
        {
            const int r7 = c16 & 7;
            const unsigned kb0 = ldsb + L_SLOT + c * 16384 + (c16 >> 3) * 4096 + r7 * 128 + ((gq ^ r7) * 16), kb1 = kb0 ^ 64;
#pragma unroll
            for (int t = 0; t < 2; ++t)
#pragma unroll
                for (int ds = 0; ds < 8; ++ds)
                    kf[t * 8 + ds] = *(LAS u32x4*)(size_t)(((ds & 1) ? kb1 : kb0) + (ds >> 1) * 1024 + t * 8192);
#pragma unroll
            for (int i = 0; i < 16; ++i) asm volatile("" : "+v"(kf[i]));
            asm volatile("" ::: "memory");
            if (lane == 0) flag[F_KREL + c] = 1u;
        }
        lds_wait_ge(flag + F_QRDY, 1, true);
        STAMP(2);
        const int hd = (c16 >> 2) & 1;
        f32x4 s0 = {0, 0, 0, 0}, s1 = {0, 0, 0, 0};
        {
            const unsigned qa = ldsb + L_Q + hd * 576 + gq * 16;
            u32x4 qf[8];
#pragma unroll
            for (int ds = 0; ds < 8; ++ds) qf[ds] = *(LAS u32x4*)(size_t)(qa + ds * 64);
#pragma unroll
            for (int ds = 0; ds < 8; ++ds) { s0 = mfma32(kf[ds], qf[ds], s0); s1 = mfma32(kf[8 + ds], qf[ds], s1); }
        }
        constexpr float C2 = 0.0625f * 1.4426950408889634f;
        float sv[8] = {s0[0] * C2, s0[1] * C2, s0[2] * C2, s0[3] * C2, s1[0] * C2, s1[1] * C2, s1[2] * C2, s1[3] * C2};
        float mx = fmaxf(fmaxf(fmaxf(sv[0], sv[1]), fmaxf(sv[2], sv[3])), fmaxf(fmaxf(sv[4], sv[5]), fmaxf(sv[6], sv[7])));
        mx = swap32_max(swap16_max(mx));
        float p[8], ls = 0.f;
#pragma unroll
        for (int i = 0; i < 8; ++i) { p[i] = __builtin_amdgcn_exp2f(sv[i] - mx); ls += p[i]; }
        ls = swap16_add(ls, ls); ls = swap32_add(ls, ls);
        {
            const u32x4 pf = {cvtpk(p[0], p[1]), cvtpk(p[2], p[3]), cvtpk(p[4], p[5]), cvtpk(p[6], p[7])};
            *(LAS u32x4*)(size_t)(ldsb + L_P + c * 1024 + lane * 16) = pf;
            if (lane == 0 || lane == 4) {
                ((volatile LAS float*)(size_t)(ldsb + L_M))[hd * 8 + c] = mx;
                ((volatile LAS float*)(size_t)(ldsb + L_L))[hd * 8 + c] = ls;
            }
            asm volatile("s_waitcnt lgkmcnt(0)" ::: "memory");
            if (lane == 0) __hip_atomic_fetch_add((LAS unsigned*)(flag + F_PCNT), 1u, __ATOMIC_RELAXED, __HIP_MEMORY_SCOPE_WORKGROUP);
        }
        STAMP(3);
        lds_wait_ge(flag + F_PCNT, 8, false);
        f32x4 o0 = {0, 0, 0, 0}, o1 = {0, 0, 0, 0};
        float M, Lw;
        {
            const f32x4 ma = *(LAS f32x4*)(size_t)(ldsb + L_M + hd * 32), mb = *(LAS f32x4*)(size_t)(ldsb + L_M + hd * 32 + 16);
            const f32x4 la = *(LAS f32x4*)(size_t)(ldsb + L_L + hd * 32), lb = *(LAS f32x4*)(size_t)(ldsb + L_L + hd * 32 + 16);
            M = fmaxf(fmaxf(fmaxf(ma[0], ma[1]), fmaxf(ma[2], ma[3])), fmaxf(fmaxf(mb[0], mb[1]), fmaxf(mb[2], mb[3])));
            Lw = 0.f;
#pragma unroll
            for (int i = 0; i < 4; ++i) { Lw = fmaf(la[i], __builtin_amdgcn_exp2f(ma[i] - M), Lw); Lw = fmaf(lb[i], __builtin_amdgcn_exp2f(mb[i] - M), Lw); }
            const int r7 = 4 * (gq & 1) + (c16 >> 2), pp = lane & 3;
            unsigned ab = ldsb + L_SLOT + ((gq >> 1) * 4 + (c >> 1)) * 1024 + r7 * 128 + (((4 * (c & 1) + (pp >> 1)) ^ r7) * 16) + 8 * (pp & 1);
            unsigned pad = ldsb + L_P + lane * 16, mad = ldsb + L_M + hd * 32;
#pragma unroll 1
            for (int ks = 0; ks < 8; ++ks) {
                const u32x4 pa = *(LAS u32x4*)(size_t)pad;
                const float f = __builtin_amdgcn_exp2f(*(LAS float*)(size_t)mad - M);
                lds_wait_ge(flag + F_LAND + (ks & 1), 80 + 16 * (ks >> 1), false);
                const u32x4 b0 = cat2(trread(ab), trread(ab + 8192)), b1 = cat2(trread(ab ^ 32), trread((ab ^ 32) + 8192));
                const f32x4 d0 = mfma32(b0, pa, f32x4{0, 0, 0, 0}), d1 = mfma32(b1, pa, f32x4{0, 0, 0, 0});
#pragma unroll
                for (int r = 0; r < 4; ++r) { o0[r] = fmaf(f, d0[r], o0[r]); o1[r] = fmaf(f, d1[r], o1[r]); }
                ab += 16384; pad += 1024; mad += 4;
            }
        }
        STAMP(4);
        {
            const int cp = c16 >> 2, dt = cp >> 1;
            const unsigned ln = WS_PART + ((blk * 2 + (cp & 1)) * 16 + 2 * c + dt) * 128;
            const f32x4 ov = dt ? o1 : o0;
            if ((lane & 3) == 0) st16_sc1(ws, ln + gq * 16, __builtin_bit_cast(u32x4, ov));
            if ((lane & 3) == 1 && gq == 0) st8_sc1(ws, ln + 64, u32x2{__float_as_uint(M), __float_as_uint(Lw)});
            STAMP(5);
            asm volatile("s_waitcnt vmcnt(0)" ::: "memory");
            if (lane == 0) __hip_atomic_fetch_add((LAS unsigned*)(flag + F_PUB), 1u, __ATOMIC_RELAXED, __HIP_MEMORY_SCOPE_WORKGROUP);
        }
        STAMP(6);
    }
    STAMP_FLUSH();
}

extern "C" void kernel_launch(void* const* d_in, const int* in_sizes, int n_in, void* d_out, int out_size, void* d_ws, size_t ws_size, hipStream_t stream) {
    (void)hipFuncSetAttribute((const void*)fused_decode, hipFuncAttributeMaxDynamicSharedMemorySize, LDS_ALLOC);
    fused_decode<<<256, NTHR, LDS_ALLOC, stream>>>((const unsigned short*)d_in[0], (const unsigned short*)d_in[1], (const unsigned short*)d_in[2],
                                                   (const unsigned short*)d_in[3], (const unsigned short*)d_in[4], (unsigned short*)d_out, (unsigned char*)d_ws);
}
```

```cpp
#include <hip/hip_runtime.h>
#include <stdint.h>

constexpr int NWAVE = 11, NTHR = NWAVE * 64;
constexpr int LDS_ALLOC = 156160;
constexpr int WS_E = 0, WS_RDY = 4096, WS_Q = 8192, WS_PART = 106496, WS_OUTP = 630784, WS_END = WS_OUTP + 256 * 448 * 4;

__global__ __launch_bounds__(NTHR) void gqa_decode_fused(const unsigned short* x, const unsigned short* kc, const unsigned short* vc,
                                                         const unsigned short* wq, const unsigned short* wo, unsigned short* out, unsigned char* ws) {
    asm volatile(
        "s_load_dwordx8 s[4:11], %0, 0x0\n"
        "s_load_dwordx4 s[12:15], %0, 0x20\n"
        "s_load_dwordx2 s[16:17], %0, 0x30\n"
        "s_memrealtime s[22:23]\n"
        "v_readfirstlane_b32 s3, %2\n"
        "v_and_b32 v1, 63, %2\n"
        "s_and_b32 s18, %1, 7\n"
        "s_lshr_b32 s19, %1, 3\n"
        "s_lshl_b32 s20, s18, 5\n"
        "s_add_u32 s20, s20, s19\n"
        "s_lshr_b32 s3, s3, 6\n"
        "s_cmp_lt_u32 s3, 2\n"
        "s_cbranch_scc0 gqa_NOTLOADER\n"
        "v_lshrrev_b32 v2, 2, v1\n"
        "v_and_b32 v3, 3, v1\n"
        "v_lshlrev_b32 v3, 4, v3\n"
        "v_lshl_or_b32 v5, v2, 13, v3\n"
        "s_and_b32 s24, s19, 1\n"
        "s_mul_i32 s25, s24, 224\n"
        "s_lshl_b32 s26, s3, 4\n"
        "s_add_u32 s25, s25, s26\n"
        "s_lshl_b32 s25, s25, 13\n"
        "s_lshr_b32 s26, s19, 1\n"
        "s_lshl_b32 s26, s26, 6\n"
        "s_lshl_b32 s27, s18, 10\n"
        "s_add_u32 s25, s25, s26\n"
        "s_add_u32 s25, s25, s27\n"
        "s_mul_i32 s24, s24, 448\n"
        "s_lshl_b32 s26, s20, 17\n"
        "s_lshl_b32 s45, s3, 13\n"
        "s_add_u32 s26, s26, s45\n"
        "s_lshl_b32 s27, s3, 10\n"
        "s_add_u32 s27, s27, 131072\n"
        "v_lshrrev_b32 v2, 3, v1\n"
        "v_and_b32 v3, 7, v1\n"
        "v_xor_b32 v3, v3, v2\n"
        "v_lshlrev_b32 v3, 4, v3\n"
        "v_lshl_or_b32 v4, v2, 9, v3\n"
        "v_add_u32 v8, 4096, v4\n"
        "v_lshlrev_b32 v6, 4, v1\n"
        "v_mov_b32 v20, 155968\n"
        "s_lshl_b32 s28, s3, 2\n"
        "s_add_u32 s28, s28, 155904\n"
        "v_mov_b32 v21, s28\n"
        "v_mov_b32 v22, 0\n"
        "v_lshlrev_b32 v9, 2, v1\n"
        "v_add_u32 v9, 155904, v9\n"
        "s_cmp_eq_u32 s3, 0\n"
        "s_cbranch_scc0 gqa_LD_NOZERO\n"
        "s_mov_b64 exec, 0xffffffff\n"
        "ds_write_b32 v9, v22\n"
        "s_mov_b64 exec, -1\n"
        "gqa_LD_NOZERO:\n"
        "s_waitcnt lgkmcnt(0)\n"
        "s_add_u32 s42, s10, s25\n"
        "s_addc_u32 s43, s11, 0\n"
        "s_mov_b32 m0, s27\n"
        "s_mov_b32 s46, 7\n"
        "gqa_LD_WQ:\n"
        "global_load_lds_dwordx4 v5, s[42:43]\n"
        "s_addk_i32 m0, 2048\n"
        "s_add_u32 s42, s42, 262144\n"
        "s_addc_u32 s43, s43, 0\n"
        "s_sub_u32 s46, s46, 1\n"
        "s_cmp_lg_u32 s46, 0\n"
        "s_cbranch_scc1 gqa_LD_WQ\n"
        "s_add_u32 s42, s4, s24\n"
        "s_addc_u32 s43, s5, 0\n"
        "s_mov_b32 m0, 154752\n"
        "s_mov_b64 exec, 0xfffffff\n"
        "global_load_lds_dwordx4 v6, s[42:43]\n"
        "s_mov_b64 exec, -1\n"
        "s_add_u32 s42, s6, s26\n"
        "s_addc_u32 s43, s7, 0\n"
        "s_mov_b32 s41, 8\n"
        "s_mov_b32 s44, -64\n"
        "s_add_u32 s52, s22, 204\n"
        "gqa_LD_SLOT:\n"
        "s_cmp_lt_u32 s41, 72\n"
        "s_cbranch_scc1 gqa_LD_GO\n"
        "s_cmp_lg_u32 s41, 72\n"
        "s_cbranch_scc1 gqa_LD_REL\n"
        "s_add_u32 s42, s8, s26\n"
        "s_addc_u32 s43, s9, 0\n"
        "s_lshl_b32 s45, s3, 13\n"
        "gqa_LD_REL:\n"
        "s_mov_b32 s46, 1048576\n"
        "gqa_LD_RELW:\n"
        "ds_read_b32 v22, v20\n"
        "s_sub_u32 s46, s46, 1\n"
        "s_cbranch_scc1 gqa_LD_RELD\n"
        "s_waitcnt lgkmcnt(0)\n"
        "v_cmp_ne_u32 vcc, 0, v22\n"
        "s_cbranch_vccz gqa_LD_RELW\n"
        "gqa_LD_RELD:\n"
        "v_add_u32 v20, 4, v20\n"
        "gqa_LD_GO:\n"
        "s_cmp_lt_u32 s41, 24\n"
        "s_cbranch_scc1 gqa_LD_NOPACE\n"
        "gqa_LD_PACE:\n"
        "s_memrealtime s[50:51]\n"
        "s_waitcnt lgkmcnt(0)\n"
        "s_sub_u32 s53, s50, s52\n"
        "s_cmp_lt_i32 s53, 0\n"
        "s_cbranch_scc0 gqa_LD_PACED\n"
        "s_sleep 1\n"
        "s_branch gqa_LD_PACE\n"
        "gqa_LD_PACED:\n"
        "s_add_u32 s52, s52, 54\n"
        "gqa_LD_NOPACE:\n"
        "s_mov_b32 m0, s45\n"
        "s_add_u32 s45, s45, 16384\n"
        "global_load_lds_dwordx4 v4, s[42:43] nt\n"
        "s_addk_i32 m0, 896\n"
        "s_waitcnt vmcnt(16)\n"
        "global_load_lds_dwordx4 v4, s[42:43] offset:128 nt\n"
        "s_addk_i32 m0, 896\n"
        "s_waitcnt vmcnt(16)\n"
        "global_load_lds_dwordx4 v4, s[42:43] offset:256 nt\n"
        "s_addk_i32 m0, 896\n"
        "s_waitcnt vmcnt(16)\n"
        "global_load_lds_dwordx4 v4, s[42:43] offset:384 nt\n"
        "s_addk_i32 m0, 1408\n"
        "s_waitcnt vmcnt(16)\n"
        "global_load_lds_dwordx4 v8, s[42:43] nt\n"
        "s_addk_i32 m0, 896\n"
        "s_waitcnt vmcnt(16)\n"
        "global_load_lds_dwordx4 v8, s[42:43] offset:128 nt\n"
        "s_addk_i32 m0, 896\n"
        "s_waitcnt vmcnt(16)\n"
        "global_load_lds_dwordx4 v8, s[42:43] offset:256 nt\n"
        "s_addk_i32 m0, 896\n"
        "s_waitcnt vmcnt(16)\n"
        "global_load_lds_dwordx4 v8, s[42:43] offset:384 nt\n"
        "s_add_u32 s42, s42, 16384\n"
        "s_addc_u32 s43, s43, 0\n"
        "s_add_u32 s41, s41, 8\n"
        "s_sub_i32 s46, s41, 16\n"
        "s_max_i32 s44, s44, s46\n"
        "v_mov_b32 v22, s44\n"
        "s_waitcnt vmcnt(16)\n"
        "ds_write_b32 v21, v22\n"
        "s_cmp_lg_u32 s41, 16\n"
        "s_cbranch_scc1 gqa_LD_NOBAR\n"
        "s_waitcnt lgkmcnt(0)\n"
        "s_barrier\n"
        "ds_write_b32 v21, v22\n"
        "gqa_LD_NOBAR:\n"
        "s_cmp_lt_u32 s41, 136\n"
        "s_cbranch_scc1 gqa_LD_SLOT\n"
        "v_mov_b32 v9, 155904\n"
        "s_mov_b32 s46, 1048576\n"
        "gqa_Lw_1:\n"
        "ds_read_b32 v22, v9 offset:104\n"
        "s_sub_u32 s46, s46, 1\n"
        "s_cbranch_scc1 gqa_Lwd_2\n"
        "s_waitcnt lgkmcnt(0)\n"
        "v_cmp_le_i32 vcc, 1, v22\n"
        "s_cbranch_vccnz gqa_Lwd_2\n"
        "s_branch gqa_Lw_1\n"
        "gqa_Lwd_2:\n"
        "s_waitcnt lgkmcnt(0)\n"
        "s_lshl_b32 s28, s18, 9\n"
        "s_lshl_b32 s29, s19, 4\n"
        "s_add_u32 s28, s28, s29\n"
        "s_mul_i32 s28, s28, 896\n"
        "s_lshl_b32 s29, s3, 6\n"
        "s_add_u32 s28, s28, s29\n"
        "s_add_u32 s42, s12, s28\n"
        "s_addc_u32 s43, s13, 0\n"
        "v_bfe_u32 v7, v1, 1, 4\n"
        "v_mul_u32_u24 v7, 896, v7\n"
        "v_lshrrev_b32 v2, 5, v1\n"
        "v_lshl_add_u32 v7, v2, 5, v7\n"
        "v_and_b32 v2, 1, v1\n"
        "v_lshl_add_u32 v7, v2, 4, v7\n"
        "s_mov_b32 m0, s27\n"
        "s_mov_b32 s46, 7\n"
        "gqa_LD_WO:\n"
        "global_load_lds_dwordx4 v7, s[42:43]\n"
        "s_addk_i32 m0, 2048\n"
        "s_add_u32 s42, s42, 128\n"
        "s_addc_u32 s43, s43, 0\n"
        "s_sub_u32 s46, s46, 1\n"
        "s_cmp_lg_u32 s46, 0\n"
        "s_cbranch_scc1 gqa_LD_WO\n"
        "v_mov_b32 v22, 128\n"
        "s_waitcnt vmcnt(15)\n"
        "ds_write_b32 v21, v22\n"
        "v_mov_b32 v22, 136\n"
        "s_waitcnt vmcnt(7)\n"
        "ds_write_b32 v21, v22\n"
        "v_mov_b32 v22, 143\n"
        "s_waitcnt vmcnt(0)\n"
        "ds_write_b32 v21, v22\n"
        "s_endpgm\n"
        "gqa_NOTLOADER:\n"
        "v_lshrrev_b32 v2, 4, v1\n"
        "v_and_b32 v3, 15, v1\n"
        "v_mov_b32 v4, 155904\n"
        "s_barrier\n"
        "s_cmp_eq_u32 s3, 2\n"
        "s_cbranch_scc1 gqa_AUX\n"
        "s_sub_u32 s21, s3, 3\n"
        "s_getpc_b64 s[24:25]\n"
        "gqa_TPC:\n"
        "s_mov_b32 s26, gqa_STUB0-gqa_TPC\n"
        "s_cmp_eq_u32 s21, 1\n"
        "s_cselect_b32 s26, gqa_STUB1-gqa_TPC, s26\n"
        "s_cmp_eq_u32 s21, 2\n"
        "s_cselect_b32 s26, gqa_STUB2-gqa_TPC, s26\n"
        "s_cmp_eq_u32 s21, 3\n"
        "s_cselect_b32 s26, gqa_STUB3-gqa_TPC, s26\n"
        "s_cmp_eq_u32 s21, 4\n"
        "s_cselect_b32 s26, gqa_STUB4-gqa_TPC, s26\n"
        "s_cmp_eq_u32 s21, 5\n"
        "s_cselect_b32 s26, gqa_STUB5-gqa_TPC, s26\n"
        "s_cmp_eq_u32 s21, 6\n"
        "s_cselect_b32 s26, gqa_STUB6-gqa_TPC, s26\n"
        "s_cmp_lt_u32 s21, 7\n"
        "s_cbranch_scc0 gqa_TDONE\n"
        "s_add_u32 s24, s24, s26\n"
        "s_addc_u32 s25, s25, 0\n"
        "s_mov_b64 exec, 0\n"
        "s_swappc_b64 s[30:31], s[24:25]\n"
        "s_mov_b64 exec, -1\n"
        "s_branch gqa_TDONE\n"
        "gqa_RETTRAMP:\n"
        "s_setpc_b64 s[30:31]\n"
        "gqa_TDONE:\n"
        "s_lshl_b32 s24, s21, 3\n"
        "s_add_u32 s24, s24, 16\n"
        "v_and_b32 v8, 7, v3\n"
        "v_xor_b32 v9, v2, v8\n"
        "v_lshlrev_b32 v9, 4, v9\n"
        "v_lshl_or_b32 v9, v8, 7, v9\n"
        "v_lshrrev_b32 v10, 3, v3\n"
        "v_lshl_or_b32 v9, v10, 12, v9\n"
        "s_lshl_b32 s25, s21, 14\n"
        "v_add_u32 v9, s25, v9\n"
        "v_xor_b32 v10, 64, v9\n"
        "v_bfe_u32 v11, v3, 2, 1\n"
        "s_mov_b32 s46, 1048576\n"
        "gqa_Llw_3:\n"
        "ds_read_b64 v[6:7], v4 offset:0\n"
        "s_sub_u32 s46, s46, 1\n"
        "s_cbranch_scc1 gqa_Llwd_4\n"
        "s_waitcnt lgkmcnt(0)\n"
        "v_min_i32 v6, v6, v7\n"
        "v_cmp_le_i32 vcc, s24, v6\n"
        "s_cbranch_vccnz gqa_Llwd_4\n"
        "s_sleep 1\n"
        "s_branch gqa_Llw_3\n"
        "gqa_Llwd_4:\n"
        "s_waitcnt lgkmcnt(0)\n"
        "ds_read_b128 v[32:35], v9 offset:0\n"
        "ds_read_b128 v[36:39], v10 offset:0\n"
        "ds_read_b128 v[40:43], v9 offset:1024\n"
        "ds_read_b128 v[44:47], v10 offset:1024\n"
        "ds_read_b128 v[48:51], v9 offset:2048\n"
        "ds_read_b128 v[52:55], v10 offset:2048\n"
        "ds_read_b128 v[56:59], v9 offset:3072\n"
        "ds_read_b128 v[60:63], v10 offset:3072\n"
        "ds_read_b128 v[64:67], v9 offset:8192\n"
        "ds_read_b128 v[68:71], v10 offset:8192\n"
        "ds_read_b128 v[72:75], v9 offset:9216\n"
        "ds_read_b128 v[76:79], v10 offset:9216\n"
        "ds_read_b128 v[80:83], v9 offset:10240\n"
        "ds_read_b128 v[84:87], v10 offset:10240\n"
        "ds_read_b128 v[88:91], v9 offset:11264\n"
        "ds_read_b128 v[92:95], v10 offset:11264\n"
        "s_lshl_b32 s25, s21, 2\n"
        "v_mov_b32 v12, 1\n"
        "v_add_u32 v13, s25, v4\n"
        "s_mov_b64 exec, 1\n"
        "ds_write_b32 v13, v12 offset:64\n"
        "s_mov_b64 exec, -1\n"
        "v_mul_u32_u24 v14, 576, v11\n"
        "v_lshl_add_u32 v14, v2, 4, v14\n"
        "v_add_u32 v14, 153600, v14\n"
        "s_mov_b32 s46, 1048576\n"
        "gqa_Lw_5:\n"
        "ds_read_b32 v5, v4 offset:104\n"
        "s_sub_u32 s46, s46, 1\n"
        "s_cbranch_scc1 gqa_Lwd_6\n"
        "s_waitcnt lgkmcnt(0)\n"
        "v_cmp_le_i32 vcc, 1, v5\n"
        "s_cbranch_vccnz gqa_Lwd_6\n"
        "s_sleep 1\n"
        "s_branch gqa_Lw_5\n"
        "gqa_Lwd_6:\n"
        "s_waitcnt lgkmcnt(0)\n"
        "ds_read_b128 v[96:99], v14 offset:0\n"
        "ds_read_b128 v[100:103], v14 offset:64\n"
        "ds_read_b128 v[104:107], v14 offset:128\n"
        "ds_read_b128 v[108:111], v14 offset:192\n"
        "ds_read_b128 v[112:115], v14 offset:256\n"
        "ds_read_b128 v[116:119], v14 offset:320\n"
        "ds_read_b128 v[120:123], v14 offset:384\n"
        "ds_read_b128 v[124:127], v14 offset:448\n"
        "gqa_STUB0:\n"
        "s_cbranch_execz gqa_RETTRAMP\n"
        "s_waitcnt lgkmcnt(7)\n"
        "v_mfma_f32_16x16x32_bf16 v[16:19], v[32:35], v[96:99], 0\n"
        "v_mfma_f32_16x16x32_bf16 v[20:23], v[64:67], v[96:99], 0\n"
        "s_waitcnt lgkmcnt(6)\n"
        "v_mfma_f32_16x16x32_bf16 v[16:19], v[36:39], v[100:103], v[16:19]\n"
        "v_mfma_f32_16x16x32_bf16 v[20:23], v[68:71], v[100:103], v[20:23]\n"
        "s_waitcnt lgkmcnt(5)\n"
        "v_mfma_f32_16x16x32_bf16 v[16:19], v[40:43], v[104:107], v[16:19]\n"
        "v_mfma_f32_16x16x32_bf16 v[20:23], v[72:75], v[104:107], v[20:23]\n"
        "s_waitcnt lgkmcnt(4)\n"
        "v_mfma_f32_16x16x32_bf16 v[16:19], v[44:47], v[108:111], v[16:19]\n"
        "v_mfma_f32_16x16x32_bf16 v[20:23], v[76:79], v[108:111], v[20:23]\n"
        "s_waitcnt lgkmcnt(3)\n"
        "v_mfma_f32_16x16x32_bf16 v[16:19], v[48:51], v[112:115], v[16:19]\n"
        "v_mfma_f32_16x16x32_bf16 v[20:23], v[80:83], v[112:115], v[20:23]\n"
        "s_waitcnt lgkmcnt(2)\n"
        "v_mfma_f32_16x16x32_bf16 v[16:19], v[52:55], v[116:119], v[16:19]\n"
        "v_mfma_f32_16x16x32_bf16 v[20:23], v[84:87], v[116:119], v[20:23]\n"
        "s_waitcnt lgkmcnt(1)\n"
        "v_mfma_f32_16x16x32_bf16 v[16:19], v[56:59], v[120:123], v[16:19]\n"
        "v_mfma_f32_16x16x32_bf16 v[20:23], v[88:91], v[120:123], v[20:23]\n"
        "s_waitcnt lgkmcnt(0)\n"
        "v_mfma_f32_16x16x32_bf16 v[16:19], v[60:63], v[124:127], v[16:19]\n"
        "v_mfma_f32_16x16x32_bf16 v[20:23], v[92:95], v[124:127], v[20:23]\n"
        "v_lshlrev_b32 v24, 5, v11\n"
        "s_lshl_b32 s25, s21, 2\n"
        "v_add_u32 v25, s25, v24\n"
        "v_add_u32 v25, 155776, v25\n"
        "v_add_u32 v24, 155776, v24\n"
        "v_mov_b32 v12, 1\n"
        "s_nop 3\n"
        "v_max3_f32 v26, v16, v17, v18\n"
        "v_max3_f32 v27, v20, v21, v22\n"
        "v_max3_f32 v26, v26, v19, v23\n"
        "v_max_f32 v26, v26, v27\n"
        "v_mov_b32 v27, v26\n"
        "s_nop 1\n"
        "v_permlane16_swap_b32 v26, v27\n"
        "v_max_f32 v26, v26, v27\n"
        "v_mov_b32 v27, v26\n"
        "s_nop 1\n"
        "v_permlane32_swap_b32 v26, v27\n"
        "v_max_f32 v26, v26, v27\n"
        "s_mov_b64 exec, 0x11\n"
        "ds_write_b32 v25, v26\n"
        "s_mov_b64 exec, 1\n"
        "ds_add_u32 v4, v12 offset:108\n"
        "s_mov_b64 exec, -1\n"
        "s_mov_b32 s46, 1048576\n"
        "gqa_Lw_7:\n"
        "ds_read_b32 v5, v4 offset:108\n"
        "s_sub_u32 s46, s46, 1\n"
        "s_cbranch_scc1 gqa_Lwd_8\n"
        "s_waitcnt lgkmcnt(0)\n"
        "v_cmp_le_i32 vcc, 8, v5\n"
        "s_cbranch_vccnz gqa_Lwd_8\n"
        "s_branch gqa_Lw_7\n"
        "gqa_Lwd_8:\n"
        "s_waitcnt lgkmcnt(0)\n"
        "ds_read_b128 v[28:31], v24\n"
        "ds_read_b128 v[96:99], v24 offset:16\n"
        "s_waitcnt lgkmcnt(0)\n"
        "v_max3_f32 v26, v28, v29, v30\n"
        "v_max3_f32 v27, v96, v97, v98\n"
        "v_max3_f32 v26, v26, v31, v99\n"
        "v_max_f32 v26, v26, v27\n"
        "v_mul_f32 v26, 1035512379, v26\n"
        "v_mov_b32 v27, 1035512379\n"
        "v_fma_f32 v16, v16, v27, -v26\n"
        "v_fma_f32 v17, v17, v27, -v26\n"
        "v_fma_f32 v18, v18, v27, -v26\n"
        "v_fma_f32 v19, v19, v27, -v26\n"
        "v_fma_f32 v20, v20, v27, -v26\n"
        "v_fma_f32 v21, v21, v27, -v26\n"
        "v_fma_f32 v22, v22, v27, -v26\n"
        "v_fma_f32 v23, v23, v27, -v26\n"
        "v_exp_f32 v16, v16\n"
        "v_exp_f32 v17, v17\n"
        "v_exp_f32 v18, v18\n"
        "v_exp_f32 v19, v19\n"
        "v_exp_f32 v20, v20\n"
        "v_exp_f32 v21, v21\n"
        "v_exp_f32 v22, v22\n"
        "v_exp_f32 v23, v23\n"
        "v_lshlrev_b32 v13, 4, v1\n"
        "s_lshl_b32 s25, s21, 10\n"
        "s_add_u32 s25, s25, 145408\n"
        "v_add_u32 v28, s25, v13\n"
        "v_cvt_pk_bf16_f32 v96, v16, v17\n"
        "v_cvt_pk_bf16_f32 v97, v18, v19\n"
        "v_cvt_pk_bf16_f32 v98, v20, v21\n"
        "v_cvt_pk_bf16_f32 v99, v22, v23\n"
        "v_add_f32 v16, v16, v17\n"
        "v_add_f32 v18, v18, v19\n"
        "v_add_f32 v20, v20, v21\n"
        "v_add_f32 v22, v22, v23\n"
        "ds_write_b128 v28, v[96:99]\n"
        "v_add_f32 v16, v16, v18\n"
        "v_add_f32 v20, v20, v22\n"
        "v_add_f32 v16, v16, v20\n"
        "v_mov_b32 v17, v16\n"
        "s_nop 1\n"
        "v_permlane16_swap_b32 v16, v17\n"
        "v_add_f32 v16, v16, v17\n"
        "v_mov_b32 v17, v16\n"
        "s_nop 1\n"
        "v_permlane32_swap_b32 v16, v17\n"
        "v_add_f32 v16, v16, v17\n"
        "s_mov_b64 exec, 0x11\n"
        "ds_write_b32 v25, v16 offset:64\n"
        "s_mov_b64 exec, 1\n"
        "ds_add_u32 v4, v12 offset:112\n"
        "s_mov_b64 exec, -1\n"
        "v_add_u32 v28, 145408, v13\n"
        "v_and_b32 v8, 1, v2\n"
        "v_lshrrev_b32 v9, 2, v3\n"
        "v_lshl_add_u32 v8, v8, 2, v9\n"
        "v_bfe_u32 v10, v1, 1, 1\n"
        "s_and_b32 s25, s21, 1\n"
        "s_lshl_b32 s25, s25, 2\n"
        "v_or_b32 v10, s25, v10\n"
        "v_xor_b32 v10, v10, v8\n"
        "v_lshlrev_b32 v10, 4, v10\n"
        "v_lshl_or_b32 v10, v8, 7, v10\n"
        "v_and_b32 v13, 1, v1\n"
        "v_lshl_or_b32 v10, v13, 3, v10\n"
        "v_lshrrev_b32 v13, 1, v2\n"
        "v_lshlrev_b32 v13, 12, v13\n"
        "s_lshr_b32 s25, s21, 1\n"
        "s_lshl_b32 s25, s25, 10\n"
        "v_add3_u32 v29, v10, v13, s25\n"
        "v_xor_b32 v30, 32, v29\n"
        "s_lshl_b32 s25, s20, 11\n"
        "s_lshl_b32 s26, s21, 7\n"
        "s_add_u32 s25, s25, s26\n"
        "s_add_u32 s25, s25, 106496\n"
        "v_and_b32 v13, 1, v9\n"
        "v_lshlrev_b32 v13, 10, v13\n"
        "v_lshrrev_b32 v10, 1, v9\n"
        "v_lshl_or_b32 v13, v10, 6, v13\n"
        "v_lshl_or_b32 v13, v2, 4, v13\n"
        "v_add_u32 v31, s25, v13\n"
        "s_lshl_b32 s25, s20, 7\n"
        "s_add_u32 s25, s25, 73728\n"
        "v_lshlrev_b32 v13, 3, v11\n"
        "v_add_u32 v13, s25, v13\n"
        "v_cmp_le_u32 s[38:39], 8, v3\n"
        "v_mov_b32 v16, 0\n"
        "v_mov_b32 v17, 0\n"
        "v_mov_b32 v18, 0\n"
        "v_mov_b32 v19, 0\n"
        "v_mov_b32 v20, 0\n"
        "v_mov_b32 v21, 0\n"
        "v_mov_b32 v22, 0\n"
        "v_mov_b32 v23, 0\n"
        "s_mov_b32 s46, 1048576\n"
        "gqa_Lw_9:\n"
        "ds_read_b32 v5, v4 offset:112\n"
        "s_sub_u32 s46, s46, 1\n"
        "s_cbranch_scc1 gqa_Lwd_10\n"
        "s_waitcnt lgkmcnt(0)\n"
        "v_cmp_le_i32 vcc, 8, v5\n"
        "s_cbranch_vccnz gqa_Lwd_10\n"
        "s_branch gqa_Lw_9\n"
        "gqa_Lwd_10:\n"
        "s_waitcnt lgkmcnt(0)\n"
        "ds_read_b128 v[32:35], v24 offset:64\n"
        "ds_read_b128 v[36:39], v24 offset:80\n"
        "s_waitcnt lgkmcnt(0)\n"
        "v_add_f32 v32, v32, v33\n"
        "v_add_f32 v34, v34, v35\n"
        "v_add_f32 v36, v36, v37\n"
        "v_add_f32 v38, v38, v39\n"
        "v_add_f32 v32, v32, v34\n"
        "v_add_f32 v36, v36, v38\n"
        "v_add_f32 v27, v32, v36\n"
        "s_cmp_eq_u32 s21, 0\n"
        "s_cbranch_scc0 gqa_CP_NOST\n"
        "s_mov_b64 exec, 0x11\n"
        "global_store_dwordx2 v13, v[26:27], s[16:17] sc1\n"
        "s_mov_b64 exec, -1\n"
        "gqa_CP_NOST:\n"
        "s_mov_b32 s24, 80\n"
        "s_mov_b32 s34, 0x11111111\n"
        "s_mov_b32 s35, 0x11111111\n"
        "gqa_STUB1:\n"
        "s_cbranch_execz gqa_RETTRAMP\n"
        "gqa_PV_LOOP:\n"
        "ds_read_b128 v[32:35], v28\n"
        "s_mov_b32 s46, 1048576\n"
        "gqa_Llw_11:\n"
        "ds_read_b64 v[6:7], v4 offset:0\n"
        "s_sub_u32 s46, s46, 1\n"
        "s_cbranch_scc1 gqa_Llwd_12\n"
        "s_waitcnt lgkmcnt(0)\n"
        "v_min_i32 v6, v6, v7\n"
        "v_cmp_le_i32 vcc, s24, v6\n"
        "s_cbranch_vccnz gqa_Llwd_12\n"
        "s_branch gqa_Llw_11\n"
        "gqa_Llwd_12:\n"
        "s_waitcnt lgkmcnt(0)\n"
        "ds_read_b64_tr_b16 v[36:37], v29\n"
        "ds_read_b64_tr_b16 v[38:39], v29 offset:8192\n"
        "ds_read_b64_tr_b16 v[40:41], v30\n"
        "ds_read_b64_tr_b16 v[42:43], v30 offset:8192\n"
        "v_add_u32 v28, 0x400, v28\n"
        "v_add_u32 v29, 0x4000, v29\n"
        "v_add_u32 v30, 0x4000, v30\n"
        "s_add_u32 s24, s24, 8\n"
        "s_cmp_lt_u32 s24, 144\n"
        "s_waitcnt lgkmcnt(2)\n"
        "v_mfma_f32_16x16x32_bf16 v[16:19], v[36:39], v[32:35], v[16:19]\n"
        "s_waitcnt lgkmcnt(0)\n"
        "v_mfma_f32_16x16x32_bf16 v[20:23], v[40:43], v[32:35], v[20:23]\n"
        "s_cbranch_scc1 gqa_PV_LOOP\n"
        "s_nop 6\n"
        "v_cndmask_b32 v16, v16, v20, s[38:39]\n"
        "v_cndmask_b32 v17, v17, v21, s[38:39]\n"
        "v_cndmask_b32 v18, v18, v22, s[38:39]\n"
        "v_cndmask_b32 v19, v19, v23, s[38:39]\n"
        "s_mov_b64 exec, s[34:35]\n"
        "global_store_dwordx4 v31, v[16:19], s[16:17] sc1\n"
        "s_mov_b64 exec, 1\n"
        "ds_add_u32 v4, v12 offset:116\n"
        "s_mov_b64 exec, -1\n"
        "s_endpgm\n"
        "gqa_AUX:\n"
        "v_mov_b32 v8, 0\n"
        "s_waitcnt lgkmcnt(0)\n"
        "global_load_dwordx2 v[10:11], v8, s[16:17] sc1\n"
        "v_mov_b32 v12, -1\n"
        "v_mov_b32 v13, -1\n"
        "v_mov_b32 v14, -1\n"
        "v_mov_b32 v15, -1\n"
        "v_lshlrev_b32 v9, 4, v1\n"
        "s_lshl_b32 s24, s20, 11\n"
        "s_add_u32 s24, s24, 106496\n"
        "v_add_u32 v16, s24, v9\n"
        "global_store_dwordx4 v16, v[12:15], s[16:17] sc1\n"
        "global_store_dwordx4 v16, v[12:15], s[16:17] offset:1024 sc1\n"
        "s_lshl_b32 s24, s20, 7\n"
        "s_add_u32 s24, s24, 73728\n"
        "v_add_u32 v18, s24, v9\n"
        "s_mov_b64 exec, 0xff\n"
        "global_store_dwordx4 v18, v[12:15], s[16:17] sc1\n"
        "s_mov_b64 exec, -1\n"
        "s_mul_i32 s25, s20, 1792\n"
        "s_add_u32 s25, s25, 630784\n"
        "v_add_u32 v17, s25, v9\n"
        "global_store_dwordx4 v17, v[12:15], s[16:17] sc1\n"
        "s_mov_b32 exec_hi, 0xffff\n"
        "global_store_dwordx4 v17, v[12:15], s[16:17] offset:1024 sc1\n"
        "s_mov_b32 exec_hi, -1\n"
        "v_lshrrev_b32 v18, 2, v3\n"
        "v_lshl_add_u32 v18, v2, 3, v18\n"
        "v_lshlrev_b32 v18, 6, v18\n"
        "v_and_b32 v19, 3, v1\n"
        "v_lshl_add_u32 v18, v19, 3, v18\n"
        "v_add_u32 v18, 131072, v18\n"
        "v_lshlrev_b32 v19, 4, v2\n"
        "v_add_u32 v19, 154752, v19\n"
        "s_mov_b32 s34, 0xffff0000\n"
        "s_mov_b32 s35, 0xffff0000\n"
        "s_mov_b32 s46, 1048576\n"
        "gqa_Llw_13:\n"
        "ds_read_b64 v[6:7], v4 offset:0\n"
        "s_sub_u32 s46, s46, 1\n"
        "s_cbranch_scc1 gqa_Llwd_14\n"
        "s_waitcnt lgkmcnt(0)\n"
        "v_min_i32 v6, v6, v7\n"
        "v_cmp_le_i32 vcc, 8, v6\n"
        "s_cbranch_vccnz gqa_Llwd_14\n"
        "s_sleep 1\n"
        "s_branch gqa_Llw_13\n"
        "gqa_Llwd_14:\n"
        "s_waitcnt lgkmcnt(0)\n"
        "ds_read_b64_tr_b16 v[32:33], v18 offset:0\n"
        "ds_read_b64_tr_b16 v[34:35], v18 offset:256\n"
        "ds_read_b64_tr_b16 v[36:37], v18 offset:32\n"
        "ds_read_b64_tr_b16 v[38:39], v18 offset:288\n"
        "ds_read_b128 v[88:91], v19 offset:0\n"
        "ds_read_b64_tr_b16 v[40:41], v18 offset:2048\n"
        "ds_read_b64_tr_b16 v[42:43], v18 offset:2304\n"
        "ds_read_b64_tr_b16 v[44:45], v18 offset:2080\n"
        "ds_read_b64_tr_b16 v[46:47], v18 offset:2336\n"
        "ds_read_b128 v[92:95], v19 offset:64\n"
        "ds_read_b64_tr_b16 v[48:49], v18 offset:4096\n"
        "ds_read_b64_tr_b16 v[50:51], v18 offset:4352\n"
        "ds_read_b64_tr_b16 v[52:53], v18 offset:4128\n"
        "ds_read_b64_tr_b16 v[54:55], v18 offset:4384\n"
        "ds_read_b128 v[96:99], v19 offset:128\n"
        "ds_read_b64_tr_b16 v[56:57], v18 offset:6144\n"
        "ds_read_b64_tr_b16 v[58:59], v18 offset:6400\n"
        "ds_read_b64_tr_b16 v[60:61], v18 offset:6176\n"
        "ds_read_b64_tr_b16 v[62:63], v18 offset:6432\n"
        "ds_read_b128 v[100:103], v19 offset:192\n"
        "ds_read_b64_tr_b16 v[64:65], v18 offset:8192\n"
        "ds_read_b64_tr_b16 v[66:67], v18 offset:8448\n"
        "ds_read_b64_tr_b16 v[68:69], v18 offset:8224\n"
        "ds_read_b64_tr_b16 v[70:71], v18 offset:8480\n"
        "ds_read_b128 v[104:107], v19 offset:256\n"
        "ds_read_b64_tr_b16 v[72:73], v18 offset:10240\n"
        "ds_read_b64_tr_b16 v[74:75], v18 offset:10496\n"
        "ds_read_b64_tr_b16 v[76:77], v18 offset:10272\n"
        "ds_read_b64_tr_b16 v[78:79], v18 offset:10528\n"
        "ds_read_b128 v[108:111], v19 offset:320\n"
        "ds_read_b64_tr_b16 v[80:81], v18 offset:12288\n"
        "ds_read_b64_tr_b16 v[82:83], v18 offset:12544\n"
        "ds_read_b64_tr_b16 v[84:85], v18 offset:12320\n"
        "ds_read_b64_tr_b16 v[86:87], v18 offset:12576\n"
        "ds_read_b128 v[112:115], v19 offset:384\n"
        "gqa_STUB2:\n"
        "s_cbranch_execz gqa_RETTRAMP\n"
        "s_waitcnt lgkmcnt(0)\n"
        "v_mfma_f32_16x16x32_bf16 v[20:23], v[88:91], v[32:35], 0\n"
        "v_mfma_f32_16x16x32_bf16 v[24:27], v[88:91], v[36:39], 0\n"
        "v_mfma_f32_16x16x32_bf16 v[20:23], v[92:95], v[40:43], v[20:23]\n"
        "v_mfma_f32_16x16x32_bf16 v[24:27], v[92:95], v[44:47], v[24:27]\n"
        "v_mfma_f32_16x16x32_bf16 v[20:23], v[96:99], v[48:51], v[20:23]\n"
        "v_mfma_f32_16x16x32_bf16 v[24:27], v[96:99], v[52:55], v[24:27]\n"
        "v_mfma_f32_16x16x32_bf16 v[20:23], v[100:103], v[56:59], v[20:23]\n"
        "v_mfma_f32_16x16x32_bf16 v[24:27], v[100:103], v[60:63], v[24:27]\n"
        "v_mfma_f32_16x16x32_bf16 v[20:23], v[104:107], v[64:67], v[20:23]\n"
        "v_mfma_f32_16x16x32_bf16 v[24:27], v[104:107], v[68:71], v[24:27]\n"
        "v_mfma_f32_16x16x32_bf16 v[20:23], v[108:111], v[72:75], v[20:23]\n"
        "v_mfma_f32_16x16x32_bf16 v[24:27], v[108:111], v[76:79], v[24:27]\n"
        "v_mfma_f32_16x16x32_bf16 v[20:23], v[112:115], v[80:83], v[20:23]\n"
        "v_mfma_f32_16x16x32_bf16 v[24:27], v[112:115], v[84:87], v[24:27]\n"
        "s_waitcnt vmcnt(0)\n"
        "v_readfirstlane_b32 s38, v10\n"
        "v_readfirstlane_b32 s39, v11\n"
        "s_lshl_b32 s24, s18, 13\n"
        "s_lshl_b32 s25, s19, 8\n"
        "s_add_u32 s24, s24, s25\n"
        "s_add_u32 s24, s24, 8192\n"
        "v_lshlrev_b32 v28, 3, v1\n"
        "s_add_u32 s40, s38, 1\n"
        "s_addc_u32 s41, s39, 0\n"
        "s_and_b32 s36, s40, 0xff\n"
        "s_lshr_b64 s[42:43], s[40:41], 8\n"
        "s_mov_b32 s37, s42\n"
        "v_add_u32 v28, s24, v28\n"
        "v_cndmask_b32 v30, v20, v24, s[34:35]\n"
        "v_add_u32 v30, 0x80, v30\n"
        "v_and_b32 v30, 0xffffff00, v30\n"
        "v_or_b32 v30, s36, v30\n"
        "v_mov_b32 v31, s37\n"
        "s_mov_b32 exec_hi, 0\n"
        "global_store_dwordx2 v28, v[30:31], s[16:17] sc1\n"
        "s_mov_b32 exec_hi, -1\n"
        "s_lshl_b32 s24, s18, 13\n"
        "s_add_u32 s24, s24, 8192\n"
        "v_lshlrev_b32 v28, 9, v2\n"
        "v_lshl_add_u32 v28, v3, 4, v28\n"
        "v_add_u32 v28, s24, v28\n"
        "v_add_u32 v29, 0x1000, v28\n"
        "s_waitcnt vmcnt(0)\n"
        "gqa_AX_QT:\n"
        "s_memrealtime s[48:49]\n"
        "s_waitcnt lgkmcnt(0)\n"
        "s_sub_u32 s48, s48, s22\n"
        "s_cmp_lt_i32 s48, 460\n"
        "s_cbranch_scc0 gqa_AX_QG0\n"
        "s_sleep 2\n"
        "s_branch gqa_AX_QT\n"
        "gqa_AX_QG0:\n"
        "s_mov_b32 s46, 32768\n"
        "gqa_AX_QG:\n"
        "global_load_dwordx4 v[32:35], v28, s[16:17] sc1\n"
        "global_load_dwordx4 v[48:51], v28, s[16:17] offset:256 sc1\n"
        "global_load_dwordx4 v[36:39], v28, s[16:17] offset:2048 sc1\n"
        "global_load_dwordx4 v[52:55], v28, s[16:17] offset:2304 sc1\n"
        "global_load_dwordx4 v[40:43], v29, s[16:17] sc1\n"
        "global_load_dwordx4 v[56:59], v29, s[16:17] offset:256 sc1\n"
        "global_load_dwordx4 v[44:47], v29, s[16:17] offset:2048 sc1\n"
        "global_load_dwordx4 v[60:63], v29, s[16:17] offset:2304 sc1\n"
        "s_waitcnt vmcnt(0)\n"
        "v_bitop3_b32 v64, v32, v34, s36 bitop3:0x7e\n"
        "v_bitop3_b32 v72, v33, v35, s37 bitop3:0x7e\n"
        "v_bitop3_b32 v65, v36, v38, s36 bitop3:0x7e\n"
        "v_bitop3_b32 v73, v37, v39, s37 bitop3:0x7e\n"
        "v_bitop3_b32 v66, v40, v42, s36 bitop3:0x7e\n"
        "v_bitop3_b32 v74, v41, v43, s37 bitop3:0x7e\n"
        "v_bitop3_b32 v67, v44, v46, s36 bitop3:0x7e\n"
        "v_bitop3_b32 v75, v45, v47, s37 bitop3:0x7e\n"
        "v_bitop3_b32 v68, v48, v50, s36 bitop3:0x7e\n"
        "v_bitop3_b32 v76, v49, v51, s37 bitop3:0x7e\n"
        "v_bitop3_b32 v69, v52, v54, s36 bitop3:0x7e\n"
        "v_bitop3_b32 v77, v53, v55, s37 bitop3:0x7e\n"
        "v_bitop3_b32 v70, v56, v58, s36 bitop3:0x7e\n"
        "v_bitop3_b32 v78, v57, v59, s37 bitop3:0x7e\n"
        "v_bitop3_b32 v71, v60, v62, s36 bitop3:0x7e\n"
        "v_bitop3_b32 v79, v61, v63, s37 bitop3:0x7e\n"
        "v_or3_b32 v64, v64, v65, v66\n"
        "v_or3_b32 v67, v67, v68, v69\n"
        "v_or3_b32 v64, v64, v70, v71\n"
        "v_or_b32 v64, v64, v67\n"
        "v_and_b32 v64, 0xff, v64\n"
        "v_or3_b32 v72, v72, v73, v74\n"
        "v_or3_b32 v75, v75, v76, v77\n"
        "v_or3_b32 v72, v72, v78, v79\n"
        "v_or3_b32 v64, v64, v72, v75\n"
        "v_cmp_ne_u32 vcc, 0, v64\n"
        "s_mov_b64 exec, vcc\n"
        "s_cbranch_execz gqa_AX_QGD\n"
        "s_sub_u32 s46, s46, 1\n"
        "s_cbranch_scc1 gqa_AX_QGD\n"
        "s_sleep 16\n"
        "s_branch gqa_AX_QG\n"
        "gqa_AX_QGD:\n"
        "s_mov_b64 exec, -1\n"
        "gqa_STUB3:\n"
        "s_cbranch_execz gqa_RETTRAMP\n"
        "s_cmp_eq_u32 s19, 0\n"
        "s_cbranch_scc0 gqa_AX_NORDY\n"
        "s_lshl_b32 s24, s18, 7\n"
        "s_add_u32 s24, s24, 4096\n"
        "v_mov_b32 v64, s24\n"
        "v_mov_b32 v66, s36\n"
        "v_mov_b32 v67, s37\n"
        "s_mov_b64 exec, 1\n"
        "global_store_dwordx2 v64, v[66:67], s[16:17] sc1\n"
        "s_mov_b64 exec, -1\n"
        "gqa_AX_NORDY:\n"
        "v_lshlrev_b32 v64, 2, v1\n"
        "v_add_u32 v64, 153600, v64\n"
        "v_and_b32 v32, 0xffffff00, v32\n"
        "v_and_b32 v48, 0xffffff00, v48\n"
        "v_and_b32 v34, 0xffffff00, v34\n"
        "v_and_b32 v50, 0xffffff00, v50\n"
        "v_add_f32 v32, v32, v48\n"
        "v_add_f32 v34, v34, v50\n"
        "v_cvt_pk_bf16_f32 v32, v32, v34\n"
        "ds_write_b32 v64, v32 offset:0\n"
        "v_and_b32 v36, 0xffffff00, v36\n"
        "v_and_b32 v52, 0xffffff00, v52\n"
        "v_and_b32 v38, 0xffffff00, v38\n"
        "v_and_b32 v54, 0xffffff00, v54\n"
        "v_add_f32 v36, v36, v52\n"
        "v_add_f32 v38, v38, v54\n"
        "v_cvt_pk_bf16_f32 v36, v36, v38\n"
        "ds_write_b32 v64, v36 offset:256\n"
        "v_and_b32 v40, 0xffffff00, v40\n"
        "v_and_b32 v56, 0xffffff00, v56\n"
        "v_and_b32 v42, 0xffffff00, v42\n"
        "v_and_b32 v58, 0xffffff00, v58\n"
        "v_add_f32 v40, v40, v56\n"
        "v_add_f32 v42, v42, v58\n"
        "v_cvt_pk_bf16_f32 v40, v40, v42\n"
        "ds_write_b32 v64, v40 offset:576\n"
        "v_and_b32 v44, 0xffffff00, v44\n"
        "v_and_b32 v60, 0xffffff00, v60\n"
        "v_and_b32 v46, 0xffffff00, v46\n"
        "v_and_b32 v62, 0xffffff00, v62\n"
        "v_add_f32 v44, v44, v60\n"
        "v_add_f32 v46, v46, v62\n"
        "v_cvt_pk_bf16_f32 v44, v44, v46\n"
        "ds_write_b32 v64, v44 offset:832\n"
        "v_mov_b32 v12, 1\n"
        "s_mov_b64 exec, 1\n"
        "ds_write_b32 v4, v12 offset:104\n"
        "s_mov_b64 exec, -1\n"
        "s_lshr_b32 s26, s19, 4\n"
        "s_and_b32 s27, s19, 15\n"
        "s_lshl_b32 s24, s18, 16\n"
        "s_lshl_b32 s25, s26, 10\n"
        "s_add_u32 s24, s24, s25\n"
        "s_lshl_b32 s25, s27, 6\n"
        "s_add_u32 s24, s24, s25\n"
        "s_add_u32 s24, s24, 106496\n"
        "v_and_b32 v8, 1, v2\n"
        "v_lshrrev_b32 v9, 1, v2\n"
        "v_lshl_or_b32 v10, v8, 1, v9\n"
        "v_lshlrev_b32 v102, 11, v3\n"
        "v_lshl_add_u32 v102, v10, 4, v102\n"
        "v_add_u32 v102, s24, v102\n"
        "v_add_u32 v103, 0x8000, v102\n"
        "s_lshl_b32 s24, s18, 12\n"
        "s_lshl_b32 s25, s26, 3\n"
        "s_add_u32 s24, s24, s25\n"
        "s_add_u32 s24, s24, 73728\n"
        "v_lshlrev_b32 v100, 7, v3\n"
        "v_add_u32 v100, s24, v100\n"
        "v_lshrrev_b32 v10, 2, v3\n"
        "v_cmp_eq_u32 s[54:55], v10, v9\n"
        "v_add_u32 v11, 2, v9\n"
        "v_cmp_eq_u32 s[56:57], v10, v11\n"
        "s_mul_i32 s25, s20, 1792\n"
        "s_add_u32 s25, s25, 630784\n"
        "v_lshlrev_b32 v104, 2, v1\n"
        "v_add_u32 v104, s25, v104\n"
        "v_lshl_add_u32 v11, v8, 3, v10\n"
        "v_lshlrev_b32 v11, 5, v11\n"
        "v_lshl_or_b32 v11, v9, 9, v11\n"
        "v_and_b32 v12, 3, v1\n"
        "v_lshl_or_b32 v11, v12, 3, v11\n"
        "v_add_u32 v105, 131072, v11\n"
        "v_and_b32 v12, 7, v1\n"
        "v_lshlrev_b32 v12, 7, v12\n"
        "v_add_u32 v106, 4096, v12\n"
        "s_mov_b32 s46, 1048576\n"
        "gqa_Lw_15:\n"
        "ds_read_b32 v5, v4 offset:112\n"
        "s_sub_u32 s46, s46, 1\n"
        "s_cbranch_scc1 gqa_Lwd_16\n"
        "s_waitcnt lgkmcnt(0)\n"
        "v_cmp_le_i32 vcc, 8, v5\n"
        "s_cbranch_vccnz gqa_Lwd_16\n"
        "s_sleep 1\n"
        "s_branch gqa_Lw_15\n"
        "gqa_Lwd_16:\n"
        "s_waitcnt lgkmcnt(0)\n"
        "s_sleep 16\n"
        "s_mov_b32 s46, 32768\n"
        "gqa_AX_RD:\n"
        "global_load_dwordx2 v[12:13], v106, s[16:17] sc1\n"
        "s_waitcnt vmcnt(0)\n"
        "v_xor_b32 v12, s36, v12\n"
        "v_xor_b32 v13, s37, v13\n"
        "v_or_b32 v12, v12, v13\n"
        "v_cmp_ne_u32 vcc, 0, v12\n"
        "s_mov_b64 exec, vcc\n"
        "s_cbranch_execz gqa_AX_RDD\n"
        "s_sub_u32 s46, s46, 1\n"
        "s_cbranch_scc1 gqa_AX_RDD\n"
        "s_sleep 16\n"
        "s_branch gqa_AX_RD\n"
        "gqa_AX_RDD:\n"
        "s_mov_b64 exec, -1\n"
        "s_cmp_eq_u32 s20, 0\n"
        "s_cbranch_scc0 gqa_AX_NOE\n"
        "v_mov_b32 v8, 0\n"
        "v_mov_b32 v10, s40\n"
        "v_mov_b32 v11, s41\n"
        "s_mov_b64 exec, 1\n"
        "global_store_dwordx2 v8, v[10:11], s[16:17] sc1\n"
        "s_mov_b64 exec, -1\n"
        "gqa_AX_NOE:\n"
        "s_lshr_b32 s24, s19, 3\n"
        "s_lshl_b32 s24, s24, 3\n"
        "s_add_u32 s28, s18, s24\n"
        "s_lshl_b32 s24, s28, 5\n"
        "s_and_b32 s25, s19, 7\n"
        "s_lshl_b32 s25, s25, 2\n"
        "s_add_u32 s29, s24, s25\n"
        "s_lshl_b32 s24, s29, 2\n"
        "s_add_u32 s24, s24, 630784\n"
        "v_mul_u32_u24 v108, 1792, v1\n"
        "v_add_u32 v108, s24, v108\n"
        "v_add_u32 v109, 0x1c000, v108\n"
        "v_add_u32 v110, 0x38000, v108\n"
        "v_add_u32 v111, 0x54000, v108\n"
        "v_and_b32 v8, 1, v2\n"
        "v_lshrrev_b32 v9, 1, v2\n"
        "v_lshl_add_u32 v8, v8, 1, v9\n"
        "v_add_u32 v8, s29, v8\n"
        "v_lshlrev_b32 v107, 1, v8\n"
        "s_mov_b32 s46, 1048576\n"
        "gqa_Llw_17:\n"
        "ds_read_b64 v[6:7], v4 offset:0\n"
        "s_sub_u32 s46, s46, 1\n"
        "s_cbranch_scc1 gqa_Llwd_18\n"
        "s_waitcnt lgkmcnt(0)\n"
        "v_min_i32 v6, v6, v7\n"
        "v_cmp_le_i32 vcc, 104, v6\n"
        "s_cbranch_vccnz gqa_Llwd_18\n"
        "s_sleep 1\n"
        "s_branch gqa_Llw_17\n"
        "gqa_Llwd_18:\n"
        "s_waitcnt lgkmcnt(0)\n"
        "s_mov_b32 s46, 32768\n"
        "gqa_AX_ST:\n"
        "global_load_dwordx2 v[96:97], v100, s[16:17] sc1\n"
        "global_load_dwordx2 v[98:99], v100, s[16:17] offset:2048 sc1\n"
        "s_waitcnt vmcnt(0)\n"
        "v_max3_u32 v8, v96, v97, v98\n"
        "v_max_u32 v8, v8, v99\n"
        "v_cmp_eq_u32 vcc, -1, v8\n"
        "s_mov_b64 exec, vcc\n"
        "s_cbranch_execz gqa_AX_STD\n"
        "s_sub_u32 s46, s46, 1\n"
        "s_cbranch_scc1 gqa_AX_STD\n"
        "s_sleep 16\n"
        "s_branch gqa_AX_ST\n"
        "gqa_AX_STD:\n"
        "s_mov_b64 exec, -1\n"
        "v_max_f32 v8, v96, v98\n"
        "s_nop 1\n"
        "v_max_f32_dpp v8, v8, v8 quad_perm:[1,0,3,2] row_mask:0xf bank_mask:0xf\n"
        "s_nop 1\n"
        "v_max_f32_dpp v8, v8, v8 quad_perm:[2,3,0,1] row_mask:0xf bank_mask:0xf\n"
        "s_nop 1\n"
        "v_max_f32_dpp v8, v8, v8 row_half_mirror row_mask:0xf bank_mask:0xf\n"
        "s_nop 1\n"
        "v_max_f32_dpp v8, v8, v8 row_mirror row_mask:0xf bank_mask:0xf\n"
        "s_nop 1\n"
        "v_sub_f32 v9, v96, v8\n"
        "v_sub_f32 v10, v98, v8\n"
        "v_exp_f32 v96, v9\n"
        "v_exp_f32 v98, v10\n"
        "s_nop 0\n"
        "v_mul_f32 v12, v96, v97\n"
        "v_fmac_f32 v12, v98, v99\n"
        "s_nop 1\n"
        "v_add_f32_dpp v12, v12, v12 quad_perm:[1,0,3,2] row_mask:0xf bank_mask:0xf\n"
        "s_nop 1\n"
        "v_add_f32_dpp v12, v12, v12 quad_perm:[2,3,0,1] row_mask:0xf bank_mask:0xf\n"
        "s_nop 1\n"
        "v_add_f32_dpp v12, v12, v12 row_half_mirror row_mask:0xf bank_mask:0xf\n"
        "s_nop 1\n"
        "v_add_f32_dpp v12, v12, v12 row_mirror row_mask:0xf bank_mask:0xf\n"
        "s_nop 1\n"
        "v_rcp_f32 v12, v12\n"
        "s_nop 0\n"
        "v_mul_f32 v96, v96, v12\n"
        "v_mul_f32 v98, v98, v12\n"
        "v_mov_b32 v88, -1\n"
        "v_mov_b32 v89, -1\n"
        "v_mov_b32 v90, -1\n"
        "v_mov_b32 v91, -1\n"
        "v_mov_b32 v92, -1\n"
        "v_mov_b32 v93, -1\n"
        "v_mov_b32 v94, -1\n"
        "v_mov_b32 v95, -1\n"
        "gqa_STUB4:\n"
        "s_cbranch_execz gqa_RETTRAMP\n"
        "s_mov_b32 s46, 1048576\n"
        "gqa_Lw_19:\n"
        "ds_read_b32 v5, v4 offset:116\n"
        "s_sub_u32 s46, s46, 1\n"
        "s_cbranch_scc1 gqa_Lwd_20\n"
        "s_waitcnt lgkmcnt(0)\n"
        "v_cmp_le_i32 vcc, 8, v5\n"
        "s_cbranch_vccnz gqa_Lwd_20\n"
        "s_branch gqa_Lw_19\n"
        "gqa_Lwd_20:\n"
        "s_waitcnt lgkmcnt(0)\n"
        "s_sleep 13\n"
        "s_mov_b32 s46, 32768\n"
        "s_mov_b32 s49, 0\n"
        "gqa_AX_H1:\n"
        "global_load_dwordx4 v[88:91], v102, s[16:17] sc1\n"
        "global_load_dwordx4 v[92:95], v103, s[16:17] sc1\n"
        "s_cmp_lg_u32 s49, 0\n"
        "s_cbranch_scc1 gqa_AX_H1W\n"
        "s_mov_b64 s[50:51], exec\n"
        "s_mov_b64 exec, -1\n"
        "s_mov_b32 s47, 8\n"
        "gqa_AX_WOS:\n"
        "ds_read_b64 v[6:7], v4 offset:0\n"
        "s_waitcnt lgkmcnt(0)\n"
        "v_min_i32 v6, v6, v7\n"
        "v_cmp_le_i32 vcc, 143, v6\n"
        "s_cbranch_vccnz gqa_AX_WOR\n"
        "s_sub_u32 s47, s47, 1\n"
        "s_cmp_lg_u32 s47, 0\n"
        "s_cbranch_scc1 gqa_AX_WOS\n"
        "s_branch gqa_AX_WON\n"
        "gqa_AX_WOR:\n"
        "ds_read_b64_tr_b16 v[32:33], v105 offset:0\n"
        "ds_read_b64_tr_b16 v[34:35], v105 offset:128\n"
        "ds_read_b64_tr_b16 v[36:37], v105 offset:1024\n"
        "ds_read_b64_tr_b16 v[38:39], v105 offset:1152\n"
        "ds_read_b64_tr_b16 v[40:41], v105 offset:2048\n"
        "ds_read_b64_tr_b16 v[42:43], v105 offset:2176\n"
        "ds_read_b64_tr_b16 v[44:45], v105 offset:3072\n"
        "ds_read_b64_tr_b16 v[46:47], v105 offset:3200\n"
        "ds_read_b64_tr_b16 v[48:49], v105 offset:4096\n"
        "ds_read_b64_tr_b16 v[50:51], v105 offset:4224\n"
        "ds_read_b64_tr_b16 v[52:53], v105 offset:5120\n"
        "ds_read_b64_tr_b16 v[54:55], v105 offset:5248\n"
        "ds_read_b64_tr_b16 v[56:57], v105 offset:6144\n"
        "ds_read_b64_tr_b16 v[58:59], v105 offset:6272\n"
        "ds_read_b64_tr_b16 v[60:61], v105 offset:7168\n"
        "ds_read_b64_tr_b16 v[62:63], v105 offset:7296\n"
        "ds_read_b64_tr_b16 v[64:65], v105 offset:8192\n"
        "ds_read_b64_tr_b16 v[66:67], v105 offset:8320\n"
        "ds_read_b64_tr_b16 v[68:69], v105 offset:9216\n"
        "ds_read_b64_tr_b16 v[70:71], v105 offset:9344\n"
        "ds_read_b64_tr_b16 v[72:73], v105 offset:10240\n"
        "ds_read_b64_tr_b16 v[74:75], v105 offset:10368\n"
        "ds_read_b64_tr_b16 v[76:77], v105 offset:11264\n"
        "ds_read_b64_tr_b16 v[78:79], v105 offset:11392\n"
        "ds_read_b64_tr_b16 v[80:81], v105 offset:12288\n"
        "ds_read_b64_tr_b16 v[82:83], v105 offset:12416\n"
        "ds_read_b64_tr_b16 v[84:85], v105 offset:13312\n"
        "ds_read_b64_tr_b16 v[86:87], v105 offset:13440\n"
        "s_mov_b32 s49, 1\n"
        "gqa_AX_WON:\n"
        "s_mov_b64 exec, s[50:51]\n"
        "gqa_AX_H1W:\n"
        "s_waitcnt vmcnt(0)\n"
        "v_max3_u32 v8, v88, v89, v90\n"
        "v_max3_u32 v9, v92, v93, v94\n"
        "v_max3_u32 v8, v8, v91, v95\n"
        "v_max_u32 v8, v8, v9\n"
        "v_cmp_eq_u32 vcc, -1, v8\n"
        "s_mov_b64 exec, vcc\n"
        "s_cbranch_execz gqa_AX_H1D\n"
        "s_sub_u32 s46, s46, 1\n"
        "s_cbranch_scc0 gqa_AX_H1\n"
        "gqa_AX_H1D:\n"
        "s_mov_b64 exec, -1\n"
        "s_cmp_lg_u32 s49, 0\n"
        "s_cbranch_scc1 gqa_AX_WOK\n"
        "s_mov_b32 s46, 1048576\n"
        "gqa_Llw_21:\n"
        "ds_read_b64 v[6:7], v4 offset:0\n"
        "s_sub_u32 s46, s46, 1\n"
        "s_cbranch_scc1 gqa_Llwd_22\n"
        "s_waitcnt lgkmcnt(0)\n"
        "v_min_i32 v6, v6, v7\n"
        "v_cmp_le_i32 vcc, 143, v6\n"
        "s_cbranch_vccnz gqa_Llwd_22\n"
        "s_branch gqa_Llw_21\n"
        "gqa_Llwd_22:\n"
        "s_waitcnt lgkmcnt(0)\n"
        "ds_read_b64_tr_b16 v[32:33], v105 offset:0\n"
        "ds_read_b64_tr_b16 v[34:35], v105 offset:128\n"
        "ds_read_b64_tr_b16 v[36:37], v105 offset:1024\n"
        "ds_read_b64_tr_b16 v[38:39], v105 offset:1152\n"
        "ds_read_b64_tr_b16 v[40:41], v105 offset:2048\n"
        "ds_read_b64_tr_b16 v[42:43], v105 offset:2176\n"
        "ds_read_b64_tr_b16 v[44:45], v105 offset:3072\n"
        "ds_read_b64_tr_b16 v[46:47], v105 offset:3200\n"
        "ds_read_b64_tr_b16 v[48:49], v105 offset:4096\n"
        "ds_read_b64_tr_b16 v[50:51], v105 offset:4224\n"
        "ds_read_b64_tr_b16 v[52:53], v105 offset:5120\n"
        "ds_read_b64_tr_b16 v[54:55], v105 offset:5248\n"
        "ds_read_b64_tr_b16 v[56:57], v105 offset:6144\n"
        "ds_read_b64_tr_b16 v[58:59], v105 offset:6272\n"
        "ds_read_b64_tr_b16 v[60:61], v105 offset:7168\n"
        "ds_read_b64_tr_b16 v[62:63], v105 offset:7296\n"
        "ds_read_b64_tr_b16 v[64:65], v105 offset:8192\n"
        "ds_read_b64_tr_b16 v[66:67], v105 offset:8320\n"
        "ds_read_b64_tr_b16 v[68:69], v105 offset:9216\n"
        "ds_read_b64_tr_b16 v[70:71], v105 offset:9344\n"
        "ds_read_b64_tr_b16 v[72:73], v105 offset:10240\n"
        "ds_read_b64_tr_b16 v[74:75], v105 offset:10368\n"
        "ds_read_b64_tr_b16 v[76:77], v105 offset:11264\n"
        "ds_read_b64_tr_b16 v[78:79], v105 offset:11392\n"
        "ds_read_b64_tr_b16 v[80:81], v105 offset:12288\n"
        "ds_read_b64_tr_b16 v[82:83], v105 offset:12416\n"
        "ds_read_b64_tr_b16 v[84:85], v105 offset:13312\n"
        "ds_read_b64_tr_b16 v[86:87], v105 offset:13440\n"
        "gqa_AX_WOK:\n"
        "gqa_STUB5:\n"
        "s_cbranch_execz gqa_RETTRAMP\n"
        "v_mul_f32 v13, v96, v88\n"
        "v_mul_f32 v14, v96, v89\n"
        "v_mul_f32 v15, v96, v90\n"
        "v_mul_f32 v16, v96, v91\n"
        "v_fmac_f32 v13, v98, v92\n"
        "v_fmac_f32 v14, v98, v93\n"
        "v_fmac_f32 v15, v98, v94\n"
        "v_fmac_f32 v16, v98, v95\n"
        "v_add_f32_dpp v13, v13, v13 quad_perm:[1,0,3,2] row_mask:0xf bank_mask:0xf\n"
        "v_add_f32_dpp v14, v14, v14 quad_perm:[1,0,3,2] row_mask:0xf bank_mask:0xf\n"
        "v_add_f32_dpp v15, v15, v15 quad_perm:[1,0,3,2] row_mask:0xf bank_mask:0xf\n"
        "v_add_f32_dpp v16, v16, v16 quad_perm:[1,0,3,2] row_mask:0xf bank_mask:0xf\n"
        "v_add_f32_dpp v13, v13, v13 quad_perm:[2,3,0,1] row_mask:0xf bank_mask:0xf\n"
        "v_add_f32_dpp v14, v14, v14 quad_perm:[2,3,0,1] row_mask:0xf bank_mask:0xf\n"
        "v_add_f32_dpp v15, v15, v15 quad_perm:[2,3,0,1] row_mask:0xf bank_mask:0xf\n"
        "v_add_f32_dpp v16, v16, v16 quad_perm:[2,3,0,1] row_mask:0xf bank_mask:0xf\n"
        "v_add_f32_dpp v13, v13, v13 row_half_mirror row_mask:0xf bank_mask:0xf\n"
        "v_add_f32_dpp v14, v14, v14 row_half_mirror row_mask:0xf bank_mask:0xf\n"
        "v_add_f32_dpp v15, v15, v15 row_half_mirror row_mask:0xf bank_mask:0xf\n"
        "v_add_f32_dpp v16, v16, v16 row_half_mirror row_mask:0xf bank_mask:0xf\n"
        "v_add_f32_dpp v13, v13, v13 row_mirror row_mask:0xf bank_mask:0xf\n"
        "v_add_f32_dpp v14, v14, v14 row_mirror row_mask:0xf bank_mask:0xf\n"
        "v_add_f32_dpp v15, v15, v15 row_mirror row_mask:0xf bank_mask:0xf\n"
        "v_add_f32_dpp v16, v16, v16 row_mirror row_mask:0xf bank_mask:0xf\n"
        "s_waitcnt lgkmcnt(0)\n"
        "v_cvt_pk_bf16_f32 v8, v13, v14\n"
        "v_cvt_pk_bf16_f32 v9, v15, v16\n"
        "v_mov_b32 v10, v8\n"
        "v_mov_b32 v11, v9\n"
        "s_nop 1\n"
        "v_permlane32_swap_b32 v8, v10\n"
        "v_permlane32_swap_b32 v9, v11\n"
        "v_cndmask_b32 v12, 0, v8, s[54:55]\n"
        "v_cndmask_b32 v13, 0, v9, s[54:55]\n"
        "v_cndmask_b32 v14, 0, v10, s[54:55]\n"
        "v_cndmask_b32 v15, 0, v11, s[54:55]\n"
        "v_cndmask_b32 v16, 0, v8, s[56:57]\n"
        "v_cndmask_b32 v17, 0, v9, s[56:57]\n"
        "v_cndmask_b32 v18, 0, v10, s[56:57]\n"
        "v_cndmask_b32 v19, 0, v11, s[56:57]\n"
        "s_nop 0\n"
        "v_mfma_f32_16x16x32_bf16 v[112:115], v[12:15], v[32:35], 0\n"
        "v_mfma_f32_16x16x32_bf16 v[112:115], v[16:19], v[36:39], v[112:115]\n"
        "v_mfma_f32_16x16x32_bf16 v[116:119], v[12:15], v[40:43], 0\n"
        "v_mfma_f32_16x16x32_bf16 v[116:119], v[16:19], v[44:47], v[116:119]\n"
        "v_mfma_f32_16x16x32_bf16 v[120:123], v[12:15], v[48:51], 0\n"
        "v_mfma_f32_16x16x32_bf16 v[120:123], v[16:19], v[52:55], v[120:123]\n"
        "v_mfma_f32_16x16x32_bf16 v[124:127], v[12:15], v[56:59], 0\n"
        "v_mfma_f32_16x16x32_bf16 v[124:127], v[16:19], v[60:63], v[124:127]\n"
        "v_mfma_f32_16x16x32_bf16 v[128:131], v[12:15], v[64:67], 0\n"
        "v_mfma_f32_16x16x32_bf16 v[128:131], v[16:19], v[68:71], v[128:131]\n"
        "v_mfma_f32_16x16x32_bf16 v[132:135], v[12:15], v[72:75], 0\n"
        "v_mfma_f32_16x16x32_bf16 v[132:135], v[16:19], v[76:79], v[132:135]\n"
        "global_store_dword v104, v112, s[16:17] offset:0 sc1\n"
        "v_mfma_f32_16x16x32_bf16 v[136:139], v[12:15], v[80:83], 0\n"
        "v_mfma_f32_16x16x32_bf16 v[136:139], v[16:19], v[84:87], v[136:139]\n"
        "global_store_dword v104, v116, s[16:17] offset:256 sc1\n"
        "global_store_dword v104, v120, s[16:17] offset:512 sc1\n"
        "global_store_dword v104, v124, s[16:17] offset:768 sc1\n"
        "global_store_dword v104, v128, s[16:17] offset:1024 sc1\n"
        "s_nop 1\n"
        "global_store_dword v104, v132, s[16:17] offset:1280 sc1\n"
        "s_nop 3\n"
        "global_store_dword v104, v136, s[16:17] offset:1536 sc1\n"
        "gqa_STUB6:\n"
        "s_cbranch_execz gqa_RETTRAMP\n"
        "s_cmp_lt_u32 s19, 16\n"
        "s_cbranch_scc0 gqa_AX_END\n"
        "s_cmp_lt_u32 s28, 14\n"
        "s_cbranch_scc0 gqa_AX_END\n"
        "s_mov_b32 s46, 32768\n"
        "s_sleep 13\n"
        "global_load_dwordx4 v[32:35], v108, s[16:17] sc1\n"
        "global_load_dwordx4 v[36:39], v109, s[16:17] sc1\n"
        "global_load_dwordx4 v[40:43], v110, s[16:17] sc1\n"
        "global_load_dwordx4 v[44:47], v111, s[16:17] sc1\n"
        "global_load_dwordx4 v[32:35], v108, s[16:17] sc1\n"
        "global_load_dwordx4 v[36:39], v109, s[16:17] sc1\n"
        "global_load_dwordx4 v[40:43], v110, s[16:17] sc1\n"
        "global_load_dwordx4 v[44:47], v111, s[16:17] sc1\n"
        "gqa_AX_H2:\n"
        "s_waitcnt vmcnt(4)\n"
        "v_max3_u32 v8, v32, v33, v34\n"
        "v_max3_u32 v9, v36, v37, v38\n"
        "v_max3_u32 v10, v40, v41, v42\n"
        "v_max3_u32 v11, v44, v45, v46\n"
        "v_max3_u32 v8, v8, v35, v39\n"
        "v_max3_u32 v10, v10, v43, v47\n"
        "v_max3_u32 v8, v8, v9, v11\n"
        "v_max_u32 v8, v8, v10\n"
        "v_cmp_eq_u32 vcc, -1, v8\n"
        "s_mov_b64 exec, vcc\n"
        "s_cbranch_execz gqa_AX_H2D\n"
        "s_sub_u32 s46, s46, 1\n"
        "s_cbranch_scc1 gqa_AX_H2D\n"
        "global_load_dwordx4 v[32:35], v108, s[16:17] sc1\n"
        "global_load_dwordx4 v[36:39], v109, s[16:17] sc1\n"
        "global_load_dwordx4 v[40:43], v110, s[16:17] sc1\n"
        "global_load_dwordx4 v[44:47], v111, s[16:17] sc1\n"
        "s_branch gqa_AX_H2\n"
        "gqa_AX_H2D:\n"
        "s_mov_b64 exec, -1\n"
        "v_add_f32 v48, v32, v36\n"
        "v_add_f32 v49, v33, v37\n"
        "v_add_f32 v50, v34, v38\n"
        "v_add_f32 v51, v35, v39\n"
        "v_add_f32 v52, v40, v44\n"
        "v_add_f32 v53, v41, v45\n"
        "v_add_f32 v54, v42, v46\n"
        "v_add_f32 v55, v43, v47\n"
        "v_add_f32 v48, v48, v52\n"
        "v_add_f32 v49, v49, v53\n"
        "v_add_f32 v50, v50, v54\n"
        "v_add_f32 v51, v51, v55\n"
        "s_nop 1\n"
        "v_permlane32_swap_b32 v48, v49\n"
        "v_permlane32_swap_b32 v50, v51\n"
        "v_add_f32 v48, v48, v49\n"
        "v_add_f32 v50, v50, v51\n"
        "s_nop 1\n"
        "v_permlane16_swap_b32 v48, v50\n"
        "v_add_f32 v48, v48, v50\n"
        "s_nop 1\n"
        "v_add_f32_dpp v48, v48, v48 quad_perm:[1,0,3,2] row_mask:0xf bank_mask:0xf\n"
        "s_nop 1\n"
        "v_add_f32_dpp v48, v48, v48 quad_perm:[2,3,0,1] row_mask:0xf bank_mask:0xf\n"
        "s_nop 1\n"
        "v_add_f32_dpp v48, v48, v48 row_half_mirror row_mask:0xf bank_mask:0xf\n"
        "s_nop 1\n"
        "v_add_f32_dpp v48, v48, v48 row_mirror row_mask:0xf bank_mask:0xf\n"
        "s_nop 1\n"
        "v_cvt_pk_bf16_f32 v48, v48, v48\n"
        "s_mov_b32 exec_lo, 0x00010001\n"
        "s_mov_b32 exec_hi, 0x00010001\n"
        "global_store_short v107, v48, s[14:15]\n"
        "s_mov_b64 exec, -1\n"
        "gqa_AX_END:\n"
        "s_endpgm\n"
        :: "s"(__builtin_amdgcn_kernarg_segment_ptr()), "s"(blockIdx.x), "v"(threadIdx.x)
        : "v1", "v2", "v3", "v4", "v5", "v6", "v7", "v8", "v9", "v10", "v11", "v12", "v13", "v14", "v15", "v16", "v17", "v18", "v19", "v20", "v21", "v22", "v23", "v24", "v25", "v26", "v27", "v28", "v29", "v30", "v31", "v32", "v33", "v34", "v35", "v36", "v37", "v38", "v39", "v40", "v41", "v42", "v43", "v44", "v45", "v46", "v47", "v48", "v49", "v50", "v51", "v52", "v53", "v54", "v55", "v56", "v57", "v58", "v59", "v60", "v61", "v62", "v63", "v64", "v65", "v66", "v67", "v68", "v69", "v70", "v71", "v72", "v73", "v74", "v75", "v76", "v77", "v78", "v79", "v80", "v81", "v82", "v83", "v84", "v85", "v86", "v87", "v88", "v89", "v90", "v91", "v92", "v93", "v94", "v95", "v96", "v97", "v98", "v99", "v100", "v101", "v102", "v103", "v104", "v105", "v106", "v107", "v108", "v109", "v110", "v111", "v112", "v113", "v114", "v115", "v116", "v117", "v118", "v119", "v120", "v121", "v122", "v123", "v124", "v125", "v126", "v127", "v128", "v129", "v130", "v131", "v132", "v133", "v134", "v135", "v136", "v137", "v138", "v139", "s3", "s4", "s5", "s6", "s7", "s8", "s9", "s10", "s11", "s12", "s13", "s14", "s15", "s16", "s17", "s18", "s19", "s20", "s21", "s22", "s23", "s24", "s25", "s26", "s27", "s28", "s29", "s30", "s31", "s34", "s35", "s36", "s37", "s38", "s39", "s40", "s41", "s42", "s43", "s44", "s45", "s46", "s47", "s48", "s49", "s50", "s51", "s52", "s53", "s54", "s55", "s56", "s57", "s58", "s59", "s60", "s61", "s62", "s63", "vcc", "scc", "memory");
}

extern "C" void kernel_launch(void* const* d_in, const int* in_sizes, int n_in, void* d_out, int out_size, void* d_ws, size_t ws_size, hipStream_t stream) {
    (void)hipFuncSetAttribute((const void*)gqa_decode_fused, hipFuncAttributeMaxDynamicSharedMemorySize, LDS_ALLOC);
    gqa_decode_fused<<<256, NTHR, LDS_ALLOC, stream>>>((const unsigned short*)d_in[0], (const unsigned short*)d_in[1], (const unsigned short*)d_in[2],
                                                       (const unsigned short*)d_in[3], (const unsigned short*)d_in[4], (unsigned short*)d_out, (unsigned char*)d_ws);
}
```
